```python
import torch

def _softmax(x, axis=-1):
    return torch.softmax(x, dim=axis)
def _gelu(x):
    return torch.nn.functional.gelu(x, approximate="tanh")
def _silu(x):
    return torch.nn.functional.silu(x)
def _sigmoid(x):
    return torch.sigmoid(x)
def _layernorm(x, axis=-1, eps=1e-5):
    m = x.mean(dim=axis, keepdim=True)
    v = x.var(dim=axis, keepdim=True, unbiased=False)
    return (x - m) / torch.sqrt(v + eps)
def _scatter_add(tgt, idx, src, axis=0):
    return tgt.clone().index_add_(axis, idx, src)
def _segment_sum(src, seg, n):
    out = torch.zeros((n,) + src.shape[1:], dtype=src.dtype)
    return out.index_add_(0, seg, src)
def _pad(x, pw):
    flat = tuple(v for pair in reversed(pw) for v in pair)
    return torch.nn.functional.pad(x, flat)

kernel_name = 'interleave_layers__ssm_conv_s4d__glu_gated_glu__prompt'

def setup_inputs():

    _w = (lambda a: (lambda t: t.to(torch.bfloat16) if t.is_floating_point() else t)(torch.as_tensor(a)))
    def _A_setup():
        Bsz, L, D, N = 1, 2048, 448, 256
        return {
            "u": _w((lambda *s: torch.randn(*s, dtype=torch.bfloat16) * 0.05)(Bsz, L, D)),
            "log_A_real": _w((lambda *s: torch.randn(*s, dtype=torch.bfloat16) * 3.2)(D, N) * 0.5),
            "A_imag": _w((lambda *s: torch.randn(*s, dtype=torch.bfloat16) * 0.1)(D, N) * 3.14),
            "C_real": _w((lambda *s: torch.randn(*s, dtype=torch.bfloat16) * 0.1)(D, N) / N ** 0.5),
            "C_imag": _w((lambda *s: torch.randn(*s, dtype=torch.bfloat16) * 0.1)(D, N) / N ** 0.5),
            "log_dt": _w((lambda *s: torch.randn(*s, dtype=torch.bfloat16) * 0.1)(D) * 0.5 - 4.0),
            "D_skip": _w((lambda *s: torch.randn(*s, dtype=torch.bfloat16) * 0.1)(D)),
        }
    _A_in = _A_setup()

    def _B_setup():
        B, S, D, F = 1, 2048, 448, 1792
        return {
            "x": _w((lambda *s: torch.randn(*s, dtype=torch.bfloat16) * 0.05)(B, S, D)),
            "w_gate": _w((lambda *s: torch.randn(*s, dtype=torch.bfloat16) * 0.05)(D, F)),
            "w_up": _w((lambda *s: torch.randn(*s, dtype=torch.bfloat16) * 0.05)(D, F)),
            "w_down": _w((lambda *s: torch.randn(*s, dtype=torch.bfloat16) * 0.05)(F, D)),
        }
    _B_in = _B_setup()

    return {
        "A_u": _A_in["u"],
        "A_log_A_real": _A_in["log_A_real"],
        "A_A_imag": _A_in["A_imag"],
        "A_C_real": _A_in["C_real"],
        "A_C_imag": _A_in["C_imag"],
        "A_log_dt": _A_in["log_dt"],
        "A_D_skip": _A_in["D_skip"],
        "B_x": _B_in["x"],
        "B_w_gate": _B_in["w_gate"],
        "B_w_up": _B_in["w_up"],
        "B_w_down": _B_in["w_down"],
    }

def reference(A_u, A_log_A_real, A_A_imag, A_C_real, A_C_imag, A_log_dt, A_D_skip, B_x, B_w_gate, B_w_up, B_w_down):
    def _A_ref(u, log_A_real, A_imag, C_real, C_imag, log_dt, D_skip):
        L = u.shape[1]
        dt = torch.exp(log_dt)
        A = -torch.exp(log_A_real) + 1j * A_imag
        C = (C_real + 1j * C_imag) * dt[:, None]
        pos = (lambda x, dt: x.to(dt))(torch.arange(L), log_dt.dtype)
        P = torch.exp((A * dt[:, None])[:, :, None] * pos[None, None, :])
        K = 2.0 * torch.einsum("dn,dnl->dl", C, P).real
        L2 = 2 * L
        u_t = (lambda x, *d: x.permute(*d))(u, 0, 2, 1)
        Kf = (lambda x, *a, **k: torch.fft.rfft(x.float(), *a, **k))(K, L2)
        Uf = (lambda x, *a, **k: torch.fft.rfft(x.float(), *a, **k))(u_t, L2)
        y = (lambda x, *a, **k: torch.fft.irfft(x, *a, **k).to(torch.bfloat16))(Uf * Kf[None, :, :], L2)[:, :, :L]
        y = (lambda x, *d: x.permute(*d))(y, 0, 2, 1)
        return y + u * D_skip[None, None, :]

    def _B_ref(x, w_gate, w_up, w_down):
        g = torch.einsum("bsd,df->bsf", x, w_gate)
        u = torch.einsum("bsd,df->bsf", x, w_up)
        h = _sigmoid(g) * u
        return torch.einsum("bsf,fd->bsd", h, w_down)

    x = A_u
    x = x + _A_ref(_layernorm(x, axis=-1), A_log_A_real, A_A_imag, A_C_real, A_C_imag, A_log_dt, A_D_skip)
    x = x + _B_ref(_layernorm(x, axis=-1), B_w_gate, B_w_up, B_w_down)
    x = x + _A_ref(_layernorm(x, axis=-1), A_log_A_real, A_A_imag, A_C_real, A_C_imag, A_log_dt, A_D_skip)
    x = x + _B_ref(_layernorm(x, axis=-1), B_w_gate, B_w_up, B_w_down)
    return x
```

```cpp
#include <hip/hip_runtime.h>
#include <hip/hip_bf16.h>
#include <stdint.h>
#define NEXTRA 0
typedef unsigned short u16;
constexpr int L=2048, D=448, N=256, F=1792;
__device__ __forceinline__ float b2f(u16 x){ return __uint_as_float(((unsigned)x)<<16); }
__device__ __forceinline__ u16 f2b(float f){ unsigned u=__float_as_uint(f); u += 0x7FFF + ((u>>16)&1); return (u16)(u>>16); }
__device__ __forceinline__ float rb(float f){ return b2f(f2b(f)); }

__global__ void k_kgen(const u16* lar,const u16* aim,const u16* cr,const u16* ci,const u16* ldt,float* K){
  int d=blockIdx.x;
  __shared__ float sar[N],sai[N],scr[N],sci[N];
  float dt=rb(expf(b2f(ldt[d])));
  for(int n=threadIdx.x;n<N;n+=blockDim.x){
    sar[n]=-rb(expf(b2f(lar[d*N+n])))*dt; sai[n]=b2f(aim[d*N+n])*dt;
    scr[n]=b2f(cr[d*N+n])*dt; sci[n]=b2f(ci[d*N+n])*dt;
  }
  __syncthreads();
  for(int l=threadIdx.x;l<L;l+=blockDim.x){
    float p=rb((float)l); float s=0;
    for(int n=0;n<N;n++){ float e=__expf(sar[n]*p); float sn,cs; __sincosf(sai[n]*p,&sn,&cs); s+=e*(scr[n]*cs-sci[n]*sn); }
    K[d*L+l]=2.f*s;
  }
}
__global__ void k_ln(const u16* x,u16* u){
  int l=blockIdx.x; __shared__ float red[64*8];
  float s=0; for(int d=threadIdx.x;d<D;d+=64) s+=b2f(x[l*D+d]);
  for(int o=32;o;o>>=1) s+=__shfl_xor(s,o);
  float m=s/D; float v=0; for(int d=threadIdx.x;d<D;d+=64){ float t=b2f(x[l*D+d])-m; v+=t*t; }
  for(int o=32;o;o>>=1) v+=__shfl_xor(v,o);
  v=rb(v/D); m=rb(m); float sd=rb(sqrtf(rb(v+1e-5f)));
  for(int d=threadIdx.x;d<D;d+=64) u[l*D+d]=f2b(rb(b2f(x[l*D+d])-m)/sd);
}
__global__ void k_conv(const u16* x,const u16* u,const float* K,const u16* dsk,u16* o){
  int d=blockIdx.x; __shared__ float su[L], sk[L];
  for(int l=threadIdx.x;l<L;l+=blockDim.x){ su[l]=b2f(u[l*D+d]); sk[l]=K[d*L+l]; }
  __syncthreads();
  float ds=b2f(dsk[d]);
  for(int l=threadIdx.x;l<L;l+=blockDim.x){
    float s=0; for(int j=0;j<=l;j++) s+=sk[l-j]*su[j];
    float y=rb(rb(s)+rb(su[l]*ds));
    o[l*D+d]=f2b(b2f(x[l*D+d])+y);
  }
}
__global__ void k_g1(const u16* u,const u16* wg,const u16* wu,u16* h){
  int l0=blockIdx.y*8; int f=blockIdx.x*256+threadIdx.x; __shared__ float su[8][D];
  for(int i=threadIdx.x;i<8*D;i+=256) su[i/D][i%D]=b2f(u[l0*D+i]);
  __syncthreads();
  float g[8]={0},uu[8]={0};
  for(int d=0;d<D;d++){ float a=b2f(wg[d*F+f]), b=b2f(wu[d*F+f]);
    for(int r=0;r<8;r++){ g[r]+=su[r][d]*a; uu[r]+=su[r][d]*b; } }
  for(int r=0;r<8;r++){ float gg=rb(g[r]); float sg=rb(1.f/(1.f+__expf(-gg))); h[(l0+r)*F+f]=f2b(sg*rb(uu[r])); }
}
__global__ void k_g2(const u16* x,const u16* h,const u16* wd,u16* o){
  int l0=blockIdx.x*4; int d=threadIdx.x; __shared__ float sh[4][F];
  for(int i=threadIdx.x;i<4*F;i+=D) sh[i/F][i%F]=b2f(h[l0*F+i]);
  __syncthreads();
  float a[4]={0};
  for(int f=0;f<F;f++){ float w=b2f(wd[f*D+d]); for(int r=0;r<4;r++) a[r]+=sh[r][f]*w; }
  for(int r=0;r<4;r++) o[(l0+r)*D+d]=f2b(b2f(x[(l0+r)*D+d])+rb(a[r]));
}
__global__ void k_empty(int* p){ if(p==nullptr) return; }
extern "C" void kernel_launch(void* const* d_in, const int* in_sizes, int n_in, void* d_out, int out_size, void* d_ws, size_t ws_size, hipStream_t stream){
  const u16* x0=(const u16*)d_in[0];
  char* ws=(char*)d_ws;
  float* K=(float*)ws; u16* u=(u16*)(ws+(4<<20)); u16* x1=(u16*)(ws+(8<<20)); u16* x2=(u16*)(ws+(12<<20)); u16* h=(u16*)(ws+(16<<20));
  k_kgen<<<D,256,0,stream>>>((const u16*)d_in[1],(const u16*)d_in[2],(const u16*)d_in[3],(const u16*)d_in[4],(const u16*)d_in[5],K);
  const u16* xin=x0;
  for(int it=0;it<2;it++){
    k_ln<<<L,64,0,stream>>>(xin,u);
    k_conv<<<D,256,0,stream>>>(xin,u,K,(const u16*)d_in[6],x1);
    k_ln<<<L,64,0,stream>>>(x1,u);
    k_g1<<<dim3(F/256,L/8),256,0,stream>>>(u,(const u16*)d_in[8],(const u16*)d_in[9],h);
    u16* o = it==0? x2 : (u16*)d_out;
    k_g2<<<L/4,D,0,stream>>>(x1,h,(const u16*)d_in[10],o);
    xin=x2;
  }
  for(int i=0;i<NEXTRA;i++) k_empty<<<256,256,0,stream>>>((int*)d_ws);
}
```

```cpp
#include <hip/hip_runtime.h>
#include <stdint.h>
#include <type_traits>

typedef unsigned short u16;
typedef unsigned v4u __attribute__((ext_vector_type(4)));
typedef unsigned v2u __attribute__((ext_vector_type(2)));
typedef float f32x2 __attribute__((ext_vector_type(2)));
typedef float f32x4 __attribute__((ext_vector_type(4)));
typedef float f32x16 __attribute__((ext_vector_type(16)));
typedef short s16x4 __attribute__((ext_vector_type(4)));
typedef __bf16 bf16x2 __attribute__((ext_vector_type(2)));
typedef _Float16 f16x2 __attribute__((ext_vector_type(2)));
typedef __amdgpu_buffer_rsrc_t rsrc_t;
#define LAS __attribute__((address_space(3)))
#define GAS __attribute__((address_space(1)))
#define DI __device__ __forceinline__
#define RLX_AGENT __ATOMIC_RELAXED, __HIP_MEMORY_SCOPE_AGENT

constexpr int NB = 224;
constexpr int SLAB = 448 * 64;
constexpr int WS_CTL = 0;
constexpr int WS_LOC = 7 * 16384;
constexpr int WS_ST2 = 8 * 16384;
constexpr int WS_ZERO_BYTES = 10 * 16384;
constexpr int WS_ST0 = 10 * 16384;
constexpr int WS_DBG = 12 * 16384;
constexpr int WS_X0 = 1 << 20, WS_XSTR = 2 << 20;
constexpr int WS_H = 10 << 20, WS_HSTR = 8 << 20;
constexpr int WS_WGU = 26 << 20;
constexpr int WS_WD = 30 << 20;
constexpr int WS_BYTES = 32 << 20;
enum { C_A = 0, C_CA0 = 1, C_CB0 = 2, C_CA1 = 3, C_CB1 = 4, C_XA = 5, C_XB = 6 };
constexpr int L_KC = 0, KC_COPY = 4160, KC_CH = 4 * KC_COPY;
constexpr int L_U = 2 * KC_CH, U_CH = 4608;
constexpr int L_SM = L_U + 2 * U_CH;
constexpr int L_BUF = L_SM + 4096;
constexpr int L_PS = L_BUF + 2 * SLAB;
constexpr int G2S = 9216;
constexpr int L_RES = L_BUF + 8 * G2S;
constexpr int L_VW = L_SM, VW_ROW = 1040, VW_CH = 57 * VW_ROW;
constexpr int L_XT = 0, L_WR = 9216, L_MX = L_WR + 24576, L_TP = L_U, TP_CH = 4112;
constexpr int LDS_BYTES = L_VW + 2 * VW_CH;
static_assert(L_BUF + 3 * SLAB <= LDS_BYTES && LDS_BYTES <= 163840, "lds");
static_assert(L_RES + 8192 <= L_BUF + 3 * SLAB, "lds");

DI void MF16_0(f32x4& acc, v4u a, v4u b) { asm volatile("v_mfma_f32_16x16x32_f16 %0, %1, %2, 0" : "=&v"(acc) : "v"(a), "v"(b)); }
DI void MF16(f32x4& acc, v4u a, v4u b) { asm volatile("v_mfma_f32_16x16x32_f16 %0, %1, %2, %0" : "+v"(acc) : "v"(a), "v"(b)); }
#define VMCNT(n) asm volatile("s_waitcnt vmcnt(%0)" ::"n"(n) : "memory")
#define BARRIER() asm volatile("s_waitcnt lgkmcnt(0)\n\ts_barrier" ::: "memory")
#define AINL __attribute__((always_inline))
template <class T> DI void PIN(T& x) { asm volatile("" : "+v"(x)); }

#define MFOP "v_mfma_f32_32x32x16_bf16 %0, "
DI void MF_VV(f32x16& acc, v4u a, v4u b) { asm volatile(MFOP "%1, %2, %0" : "+v"(acc) : "v"(a), "v"(b)); }
template <bool Z, int S> DI void mf_av(f32x16& acc, v4u b) {
  if (Z) asm volatile(MFOP "a[%c2:%c3], %1, 0" : "=&v"(acc) : "v"(b), "n"(4 * S), "n"(4 * S + 3));
  else asm volatile(MFOP "a[%c2:%c3], %1, %0" : "+v"(acc) : "v"(b), "n"(4 * S), "n"(4 * S + 3));
}
template <bool Z, int S> DI void mf_va(f32x16& acc, v4u a) {
  if (Z) asm volatile(MFOP "%1, a[%c2:%c3], 0" : "=&v"(acc) : "v"(a), "n"(4 * S), "n"(4 * S + 3));
  else asm volatile(MFOP "%1, a[%c2:%c3], %0" : "+v"(acc) : "v"(a), "n"(4 * S), "n"(4 * S + 3));
}
template <int S, int T> DI void mf_aa(f32x16& acc) {
  asm volatile(MFOP "a[%c1:%c2], a[%c3:%c4], %0" : "+v"(acc) : "n"(4 * S), "n"(4 * S + 3), "n"(4 * T), "n"(4 * T + 3));
}
template <int S, int K> DI void ld_slot(rsrc_t r, int voff, int soff) {
  asm volatile("buffer_load_dwordx4 a[%c0:%c1], %2, %3, %4 offen offset:%c5 sc1" ::"n"(4 * S), "n"(4 * S + 3), "v"(voff), "s"(r),
               "s"(soff + (K >> 2) * 4096), "n"((K & 3) * 1024) : "memory");
}
template <int I, int N, class Fn> DI void sfor(Fn&& f) {
  if constexpr (I < N) { f(std::integral_constant<int, I>{}); sfor<I + 1, N>(f); }
}
#define SFOR(i, lo, hi) sfor<lo, hi>([&](auto i##_) AINL { constexpr int i = decltype(i##_)::value;
#define SEND });

#define STAMP(i)
#define RSTAMP(i)

DI float bflo(unsigned u) { return __uint_as_float(u << 16); }
DI float bfhi(unsigned u) { return __uint_as_float(u & 0xffff0000u); }
DI unsigned pkbf(float a, float b) { bf16x2 v = {(__bf16)a, (__bf16)b}; return __builtin_bit_cast(unsigned, v); }
DI unsigned pkh(float a, float b) {
  unsigned r; asm("v_cvt_pk_f16_f32 %0, %1, %2" : "=v"(r) : "v"(a), "v"(b)); return r;
}
DI float rbf(float a) { return bflo(pkbf(a, a)); }
DI v2u trd(LAS char* p) { return __builtin_bit_cast(v2u, __builtin_amdgcn_ds_read_tr16_b64_v4i16((LAS s16x4*)p)); }
DI v4u cat(v2u a, v2u b) { return v4u{a.x, a.y, b.x, b.y}; }
DI v4u ld16(rsrc_t r, int voff, int soff) { return __builtin_amdgcn_raw_buffer_load_b128(r, voff, soff, 16); }
DI void st16(v4u v, rsrc_t r, int voff, int soff) { __builtin_amdgcn_raw_buffer_store_b128(v, r, voff, soff, 16); }
DI int opq(int x) { PIN(x); return x; }
DI int opqs(int x) { asm volatile("" : "+s"(x)); return x; }
DI LAS char* opq(LAS char* p) { unsigned x = (unsigned)(size_t)p; PIN(x); return (LAS char*)(size_t)x; }
DI void dma16(rsrc_t r, unsigned lds, int voff, int soff) {
  asm volatile("s_mov_b32 m0, %0\n\ts_nop 0\n\tbuffer_load_dwordx4 %1, %2, %3 offen sc1 lds" ::"s"(lds), "v"(voff), "s"(r), "s"(soff) : "memory");
}
DI void dma16g(const void* g, unsigned lds) {
  asm volatile("s_mov_b32 m0, %0\n\ts_nop 0\n\tglobal_load_lds_dwordx4 %1, off" ::"s"(lds), "v"(g) : "memory");
}
template <int C> DI float dpp_add(float v) {
  return v + __builtin_bit_cast(float, __builtin_amdgcn_update_dpp(0, __builtin_bit_cast(int, v), C, 0xf, 0xf, true));
}
DI float wave_sum63(float v) {
  v = dpp_add<0x111>(v); v = dpp_add<0x112>(v); v = dpp_add<0x114>(v); v = dpp_add<0x118>(v);
  v += __builtin_bit_cast(float, __builtin_amdgcn_update_dpp(0, __builtin_bit_cast(int, v), 0x142, 0xa, 0xf, true));
  v += __builtin_bit_cast(float, __builtin_amdgcn_update_dpp(0, __builtin_bit_cast(int, v), 0x143, 0xc, 0xf, true));
  return v;
}
template <int C, int RM> DI unsigned dpp_max(unsigned v) {
  unsigned o = (unsigned)__builtin_amdgcn_update_dpp(0, (int)v, C, RM, 0xf, true); return v > o ? v : o;
}
DI unsigned wave_max63(unsigned v) {
  v = dpp_max<0x111, 0xf>(v); v = dpp_max<0x112, 0xf>(v); v = dpp_max<0x114, 0xf>(v); v = dpp_max<0x118, 0xf>(v);
  v = dpp_max<0x142, 0xa>(v); v = dpp_max<0x143, 0xc>(v); return v;
}

DI void add_one(char* base, int voff, int w, unsigned long long mask) {
  unsigned long long keep; unsigned one;
  asm volatile("s_cmp_lg_u32 %4, 0\n\ts_cbranch_scc1 1f\n\tv_mov_b32 %1, 1\n\ts_mov_b64 %0, exec\n\ts_mov_b64 exec, %5\n\t"
               "global_atomic_add %2, %1, %3\n\ts_mov_b64 exec, %0\n1:"
               : "=&s"(keep), "=&v"(one) : "v"(voff), "s"(base), "s"(w), "s"(mask) : "scc", "memory");
}
DI void poll_ge(char* base, int voff, int w, unsigned target) {
  unsigned v, cnt;
  asm volatile("s_cmp_lg_u32 %4, 0\n\ts_cbranch_scc1 2f\n\ts_mov_b32 %1, 0x40000\n1:\n\tglobal_load_dword %0, %2, %3 sc1\n\t"
               "s_waitcnt vmcnt(0)\n\tv_cmp_gt_u32 vcc, %5, %0\n\ts_cbranch_vccz 2f\n\ts_sub_u32 %1, %1, 1\n\ts_cbranch_scc0 1b\n2:"
               : "=&v"(v), "=&s"(cnt) : "v"(voff), "s"(base), "s"(w), "s"(target) : "vcc", "scc", "memory");
}
DI void signal_global(char* ws, int c, int w, int lane, int b) { add_one(ws, WS_CTL + c * 16384 + ((lane & 7) * 8 + (b & 7)) * 256, w, 0xffull); }
DI void wait_global(char* ws, int c, int w, int lane, int b) { poll_ge(ws, WS_CTL + c * 16384 + ((b & 7) * 8 + (lane & 7)) * 256, w, NB / 8); }
DI void signal_local(char* ws, int c, int w, int lane) { add_one(ws, WS_LOC + c * 256, w, 1ull); }
DI void wait_local(char* ws, int c, int w, int lane) { poll_ge(ws, WS_LOC + c * 256, w, 14u); }

struct Cx { f32x2 r, i; };
DI Cx cmul(Cx a, Cx b) { Cx c; c.r = a.r * b.r - a.i * b.i; c.i = a.r * b.i + a.i * b.r; return c; }
DI Cx cpow(f32x2 lr, f32x2 rev, float p) {
  Cx z;
#pragma unroll
  for (int k = 0; k < 2; ++k) {
    const float m = __builtin_amdgcn_exp2f(lr[k] * p), f = __builtin_amdgcn_fractf(rev[k] * p);
    z.r[k] = m * __builtin_amdgcn_cosf(f); z.i[k] = m * __builtin_amdgcn_sinf(f);
  }
  return z;
}
DI void put_vw(LAS char* p, int row, Cx v) { *(LAS v2u*)(p + row * VW_ROW) = v2u{pkh(v.r[0], v.r[1]), pkh(v.i[0], v.i[1])}; }

template <int C0, int C1, int M0, int M1> struct ConvPlan {
  static constexpr int E0 = (C0 - (M1 - 1)) > 0 ? (C0 - (M1 - 1)) : 0, NA = C1 - M0 - E0, NBF = M1 - M0, NRD = NA + NBF;
  static constexpr int count() { int n = 0; for (int m = M0; m < M1; ++m) for (int c = C0; c < C1; ++c) if (c >= m) ++n; return n; }
  static constexpr int NM = count();
  static constexpr int mc(int k) { int n = 0; for (int m = M0; m < M1; ++m) for (int c = C0; c < C1; ++c) if (c >= m) { if (n == k) return m * 8 + c; ++n; } return 0; }
  static constexpr int ord(int k) { return k == 0 ? 0 : (k <= NA ? NBF + k - 1 : k - NA); }
};
template <int C0, int C1, int M0, int M1, bool FRESH, int R0, int NRING>
DI void conv_part(f32x4 (&acc)[8], LAS char* abase, LAS char* bbase, rsrc_t rs, int rvoff, int rsoff) {
  using P = ConvPlan<C0, C1, M0, M1>;
  v4u a[2][P::NA], bb[2][P::NBF];
  auto rd = [&](int buf, int rho, int k) AINL {
    if (k < P::NBF) bb[buf][k] = *(LAS v4u*)(bbase - 64 * rho + 192 + (M0 + k) * 512);
    else { LAS char* p = abase - 64 * rho + 192 + (7 - P::E0 - (k - P::NBF)) * 512; a[buf][k - P::NBF] = cat(*(LAS v2u*)p, *(LAS v2u*)(p + 8)); }
  };
  SFOR(k, 0, P::NRD) rd(0, 0, P::ord(k)); SEND
  SFOR(n, 0, 4 * P::NM)
    constexpr int rho = n / P::NM, k = n % P::NM, m = P::mc(k) >> 3, c = P::mc(k) & 7;
    if (FRESH && rho == 0 && m == M0) MF16_0(acc[c], a[0][c - m - P::E0], bb[0][m - M0]);
    else MF16(acc[c], a[rho & 1][c - m - P::E0], bb[rho & 1][m - M0]);
    if constexpr (rho < 3) { SFOR(x, k * P::NRD / P::NM, (k + 1) * P::NRD / P::NM) rd((rho + 1) & 1, rho + 1, P::ord(x)); SEND }
    if constexpr ((n & 3) == 0 && n / 4 < NRING) ld_slot<R0 + n / 4, R0 + n / 4>(rs, rvoff, rsoff);
  SEND
}

template <bool LAST, int half>
DI void glu_down(v4u (&wd1)[20], LAS char* sm, char* ws, u16* out, rsrc_t rs, int t0, int w0, int b0) {
  constexpr int layer = LAST ? 1 : 0;
  const int t = opq(t0), lane = t & 63, w = opqs(w0), b = opqs(b0), xcd = b & 7, jj = b >> 3;
  const int q4 = (lane & 15) >> 2, p4 = lane & 3, grp = (lane >> 4) & 1, hh = lane >> 5;
  const int pan = 2 * xcd + jj / 14, lt = 4 * xcd + jj / 7, dt = jj % 7;
  const int gsoff = WS_H + layer * WS_HSTR + (32 + lt) * 114688 + w * 28672, dvoff = opq(lane * 16);
  const int baddr = (8 * hh + q4) * 64 + (4 * grp + p4) * 8;
  f32x16 a0, a1;
  LAS char* rp = sm + L_RES + (w * 2 + half) * 1024 + baddr;
  SFOR(i, 0, 28)
    VMCNT(27 - i + half);
    if constexpr (i < 10) {
      if (LAST) { mf_va<i == 0, 2 * i + 1>(a0, wd1[2 * i]); mf_va<i == 0, 2 * i + 1>(a1, wd1[2 * i + 1]); }
      else { mf_av<i == 0, 2 * i + 1>(a0, wd1[2 * i]); mf_av<i == 0, 2 * i + 1>(a1, wd1[2 * i + 1]); }
    } else {
      constexpr int s0 = i < 14 ? 36 + 2 * i : 2 * (2 * i - 28), s1 = i < 14 ? 37 + 2 * i : 2 * (2 * i - 27);
      if (LAST) { mf_aa<s0, 2 * i + 1>(a0); mf_aa<s1, 2 * i + 1>(a1); }
      else { mf_aa<2 * i + 1, s0>(a0); mf_aa<2 * i + 1, s1>(a1); }
    }
  SEND
  const v4u xf = cat(trd(rp), trd(rp + 256));
  {
    const int k1 = opq(lane & 31) - 16 * w - 8 * hh;
    v4u id0, id1;
#pragma unroll
    for (int e = 0; e < 4; ++e) {
      id0[e] = k1 == 2 * e ? 0x3f80u : (k1 == 2 * e + 1 ? 0x3f800000u : 0u);
      id1[e] = k1 + 32 == 2 * e ? 0x3f80u : (k1 + 32 == 2 * e + 1 ? 0x3f800000u : 0u);
    }
    asm volatile("s_nop 1" : "+v"(id0), "+v"(id1));
    if (LAST) { MF_VV(a0, id0, xf); MF_VV(a1, id1, xf); }
    else { MF_VV(a0, xf, id0); MF_VV(a1, xf, id1); }
  }
  if (half == 0) {
    wait_local(ws, layer * 32 + 16 + pan, w, lane);
    BARRIER();
    SFOR(k, 0, 28) ld_slot<2 * k + 1, k>(rs, dvoff, gsoff); SEND
  }
  asm volatile("s_nop 7\n\ts_nop 3" : "+v"(a0), "+v"(a1));
  LAS char* sc = sm + L_BUF + half * 4 * G2S;
  LAS char* pw = sc + w * G2S + (LAST ? (lane & 31) * 272 + hh * 16 : (lane & 31) * 144 + hh * 16);
#pragma unroll
  for (int qd = 0; qd < 4; ++qd) {
    *(LAS f32x4*)(pw + qd * 32) = f32x4{a0[4 * qd], a0[4 * qd + 1], a0[4 * qd + 2], a0[4 * qd + 3]};
    *(LAS f32x4*)(pw + qd * 32 + (LAST ? 128 : 32 * 144)) = f32x4{a1[4 * qd], a1[4 * qd + 1], a1[4 * qd + 2], a1[4 * qd + 3]};
  }
  BARRIER();
  LAS char* pr = sc + (LAST ? (t >> 3) * 272 + (t & 7) * 32 : (t >> 2) * 144 + (t & 3) * 32);
  f32x4 y0 = *(LAS f32x4*)pr, y1 = *(LAS f32x4*)(pr + 16);
#pragma unroll
  for (int ww = 1; ww < 4; ++ww) { y0 += *(LAS f32x4*)(pr + ww * G2S); y1 += *(LAS f32x4*)(pr + ww * G2S + 16); }
  const v4u ov = {pkbf(y0[0], y0[1]), pkbf(y0[2], y0[3]), pkbf(y1[0], y1[1]), pkbf(y1[2], y1[3])};
  if (LAST) {
    *(v4u*)((char*)out + ((32 * half + lt) * 32 + (t >> 3)) * 896 + dt * 128 + (t & 7) * 16) = ov;
  } else {
    st16(ov, rs, t * 16, WS_X0 + 2 * WS_XSTR + (32 * half + lt) * SLAB + dt * 4096);
    float v[16];
#pragma unroll
    for (int e = 0; e < 4; ++e) { v[2 * e] = bflo(ov[e]); v[2 * e + 1] = bfhi(ov[e]); }
#pragma unroll
    for (int e = 0; e < 8; ++e) v[8 + e] = v[e] * v[e];
#pragma unroll
    for (int e = 0; e < 8; ++e) {
      const auto sw = __builtin_amdgcn_permlane32_swap(__float_as_uint(v[e]), __float_as_uint(v[8 + e]), false, false);
      v[e] = __uint_as_float(sw[0]) + __uint_as_float(sw[1]);
    }
#pragma unroll
    for (int e = 0; e < 4; ++e) {
      const auto sw = __builtin_amdgcn_permlane16_swap(__float_as_uint(v[e]), __float_as_uint(v[4 + e]), false, false);
      v[e] = __uint_as_float(sw[0]) + __uint_as_float(sw[1]);
    }
#pragma unroll
    for (int e = 0; e < 4; ++e) { v[e] = dpp_add<0x124>(v[e]); v[e] = dpp_add<0x128>(v[e]); PIN(v[e]); }
    const float m01 = (q4 & 1) ? v[1] : v[0], m23 = (q4 & 1) ? v[3] : v[2], mine = (q4 & 2) ? m23 : m01;
    *(LAS float*)(sm + L_SM + lane * 16 + w * 4) = mine;
    BARRIER();
    {
      const f32x4 s4 = *(LAS f32x4*)(sm + L_SM + lane * 16);
      const int l = (32 * half + lt) * 32 + 8 * p4 + 4 * grp + q4;
      asm volatile("s_cmp_lg_u32 %3, 0\n\ts_cbranch_scc1 1f\n\tglobal_atomic_add_f64 %0, %1, %2\n1:"
                   ::"v"(WS_ST2 + ((l & 7) * 256 + (l >> 3)) * 16 + hh * 8), "v"((double)((s4[0] + s4[1]) + (s4[2] + s4[3]))), "s"(ws), "s"(w) : "scc", "memory");
    }
  }
}

__global__ void __launch_bounds__(256, 1)
k_fused(const u16* __restrict__ x0, const u16* __restrict__ lar, const u16* __restrict__ aim, const u16* __restrict__ cre,
        const u16* __restrict__ cim, const u16* __restrict__ ldt, const u16* __restrict__ dsk, const u16* __restrict__ wg,
        const u16* __restrict__ wu, const u16* __restrict__ wd, u16* __restrict__ out, char* __restrict__ ws) {
  __shared__ __attribute__((aligned(16))) char smem[LDS_BYTES];
  LAS char* sm = (LAS char*)smem;
  const unsigned smb = (unsigned)(size_t)sm;
  const int t = threadIdx.x, lane = t & 63, w = __builtin_amdgcn_readfirstlane(t >> 6), b = blockIdx.x;
  const rsrc_t rs = __builtin_amdgcn_make_buffer_rsrc((void*)ws, 0, WS_BYTES, 0x00020000);
  const int ch = w >> 1;
  const int q4 = (lane & 15) >> 2, p4 = lane & 3, grp = (lane >> 4) & 1, hh = lane >> 5, i16 = lane & 15, g4 = lane >> 4;
  float kscale, dskip;
  asm volatile("" ::: "a0", "a1", "a2", "a3", "a4", "a5", "a6", "a7", "a8", "a9", "a10", "a11", "a12", "a13", "a14", "a15", "a16", "a17", "a18", "a19", "a20", "a21", "a22", "a23", "a24", "a25", "a26", "a27", "a28", "a29", "a30", "a31", "a32", "a33", "a34", "a35", "a36", "a37", "a38", "a39", "a40", "a41", "a42", "a43", "a44", "a45", "a46", "a47", "a48", "a49", "a50", "a51", "a52", "a53", "a54", "a55", "a56", "a57", "a58", "a59", "a60", "a61", "a62", "a63", "a64", "a65", "a66", "a67", "a68", "a69", "a70", "a71", "a72", "a73", "a74", "a75", "a76", "a77", "a78", "a79", "a80", "a81", "a82", "a83", "a84", "a85", "a86", "a87", "a88", "a89", "a90", "a91", "a92", "a93", "a94", "a95", "a96", "a97", "a98", "a99", "a100", "a101", "a102", "a103", "a104", "a105", "a106", "a107", "a108", "a109", "a110", "a111", "a112", "a113", "a114", "a115", "a116", "a117", "a118", "a119", "a120", "a121", "a122", "a123", "a124", "a125", "a126", "a127", "a128", "a129", "a130", "a131", "a132", "a133", "a134", "a135", "a136", "a137", "a138", "a139", "a140", "a141", "a142", "a143", "a144", "a145", "a146", "a147", "a148", "a149", "a150", "a151", "a152", "a153", "a154", "a155", "a156", "a157", "a158", "a159", "a160", "a161", "a162", "a163", "a164", "a165", "a166", "a167", "a168", "a169", "a170", "a171", "a172", "a173", "a174", "a175", "a176", "a177", "a178", "a179", "a180", "a181", "a182", "a183", "a184", "a185", "a186", "a187", "a188", "a189", "a190", "a191", "a192", "a193", "a194", "a195", "a196", "a197", "a198", "a199", "a200", "a201", "a202", "a203", "a204", "a205", "a206", "a207", "a208", "a209", "a210", "a211", "a212", "a213", "a214", "a215", "a216", "a217", "a218", "a219", "a220", "a221", "a222", "a223", "a224", "a225", "a226", "a227", "a228", "a229", "a230", "a231", "a232", "a233", "a234", "a235", "a236", "a237", "a238", "a239", "a240", "a241", "a242", "a243", "a244", "a245", "a246", "a247", "a248", "a249", "a250", "a251", "a252", "a253", "a254", "a255");
  RSTAMP(0); STAMP(0);
  {
    const int d = 2 * b + ch, n0 = 2 * ((w & 1) * 64 + lane);
    const unsigned p_lar = *(const unsigned*)(lar + d * 256 + n0), p_aim = *(const unsigned*)(aim + d * 256 + n0);
    const unsigned p_cr = *(const unsigned*)(cre + d * 256 + n0), p_ci = *(const unsigned*)(cim + d * 256 + n0);
    const unsigned p_dt = ldt[d];
    dskip = bflo(dsk[d]);
    const int lt0 = b / 7, dt0 = b % 7;
    v4u xt[2], xr[3];
#pragma unroll
    for (int k = 0; k < 2; ++k) xt[k] = *(const v4u*)((const char*)x0 + (64 * lt0 + 32 * k + (t >> 3)) * 896 + dt0 * 128 + (t & 7) * 16);
#pragma unroll
    for (int k = 0; k < 3; ++k) {
      const int rr = dt0 + 7 * (w + 4 * k);
      xr[k] = *(const v4u*)((const char*)x0 + (64 * lt0 + (rr < 64 ? rr : 0)) * 896 + (lane < 56 ? lane : 0) * 16);
    }
    int wdst[3], wlo[3], whi[3], wst[3];
#pragma unroll
    for (int k = 0; k < 3; ++k) {
      int cid = b * 4 + w + 896 * k; if (cid >= 2352) cid = 2351;
      const int mtx = cid / 784, c = cid % 784;
      const char* src; int stride;
      if (mtx < 2) {
        const int ks = c / 28, cp = c % 28; stride = 3584; src = (const char*)(mtx ? wu : wg) + 16 * ks * 3584 + cp * 128;
        const int F = 2 * cp, i0 = (ks + 28 - 7 * (F & 3)) % 28, i1 = (ks + 28 - 7 * ((F + 1) & 3)) % 28;
        wdst[k] = WS_WGU + F * 57344 + (2 * i0 + mtx) * 1024; wst[k] = 57344 + 2048 * (i1 - i0); wlo[k] = 8; whi[k] = 4;
      } else {
        const int S = c / 7, dt = c % 7; stride = 896; src = (const char*)wd + 16 * S * 896 + dt * 128;
        wdst[k] = WS_WD + ((dt * 4 + S / 28) * 56 + 2 * (S % 28)) * 1024; wst[k] = 1024; wlo[k] = 4; whi[k] = 8;
      }
#pragma unroll
      for (int h = 0; h < 2; ++h) dma16g(src + (8 * h + (lane >> 3)) * stride + (lane & 7) * 16, smb + L_WR + (w * 3 + k) * 2048 + h * 1024);
    }
    int goff[11];
#pragma unroll
    for (int k = 0; k < 11; ++k) {
      const int lag = 2048 - 8 * t - k;
      goff[k] = lag < 0 ? 2 * 2054 : 2 * (int)rbf((float)lag);
    }
#pragma unroll
    for (int k = 0; k < 2; ++k) *(LAS v4u*)(sm + L_XT + (32 * k + (t >> 3)) * 144 + (t & 7) * 16) = xt[k];
    {
      const unsigned a = p_cr & 0x7fff7fffu, c = p_ci & 0x7fff7fffu;
      unsigned m = max(max(a & 0xffffu, a >> 16), max(c & 0xffffu, c >> 16));
      m = wave_max63(m);
      if (lane == 63) *(LAS unsigned*)(sm + L_MX + 4 * w) = m;
    }
    BARRIER();
#pragma unroll
    for (int sl = 0; sl < 2; ++sl) {
      LAS char* p = sm + L_XT + (32 * sl + 8 * g4 + q4) * 144 + (16 * w + 4 * p4) * 2;
      st16(cat(trd(p), trd(p + 4 * 144)), rs, (64 * dt0 + 16 * w + i16) * 64 + g4 * 16, WS_X0 + (2 * lt0 + sl) * SLAB);
    }
#pragma unroll
    for (int k = 0; k < 3; ++k) {
      float s = 0.f, q = 0.f;
#pragma unroll
      for (int e = 0; e < 4; ++e) { const float lo = bflo(xr[k][e]), hi = bfhi(xr[k][e]); s += lo + hi; q += lo * lo + hi * hi; }
      if (lane >= 56) { s = 0.f; q = 0.f; }
      s = wave_sum63(s); q = wave_sum63(q);
      const int rr = dt0 + 7 * (w + 4 * k), l = 64 * lt0 + rr;
      if (lane == 63 && rr < 64) {
        const double ds = s, dq = q;
        const v2u a = __builtin_bit_cast(v2u, ds), c = __builtin_bit_cast(v2u, dq);
        st16(cat(a, c), rs, ((l & 7) * 256 + (l >> 3)) * 16, WS_ST0);
      }
    }
    VMCNT(0);
#pragma unroll
    for (int k = 0; k < 3; ++k)
#pragma unroll
      for (int hf = 0; hf < 2; ++hf) {
        LAS char* p = sm + L_WR + (w * 3 + k) * 2048 + (wlo[k] * hh + q4) * 128 + 64 * hf + 32 * grp + 8 * p4;
        st16(cat(trd(p), trd(p + whi[k] * 128)), rs, lane * 16, wdst[k] + hf * wst[k]);
      }
    STAMP(1);
    unsigned E = max(*(LAS unsigned*)(sm + L_MX + 4 * (w & 2)), *(LAS unsigned*)(sm + L_MX + 4 * (w & 2) + 4)) >> 7;
    E = E > 253u ? 253u : E;
    const float sc = __uint_as_float((254u - E) << 23), dt = rbf(expf(bflo(p_dt)));
    kscale = 2.f * dt * __uint_as_float(E << 23);
    const f32x2 lr = {-rbf(expf(bflo(p_lar))) * dt * 1.44269504f, -rbf(expf(bfhi(p_lar))) * dt * 1.44269504f};
    const f32x2 rev = {bflo(p_aim) * dt * 0.159154943f, bfhi(p_aim) * dt * 0.159154943f};
    const Cx z1 = cpow(lr, rev, 1.f), z8 = cpow(lr, rev, 8.f), z128 = cpow(lr, rev, 128.f);
    LAS char* vw = opq(sm + L_VW + ch * VW_CH + 8 * ((w & 1) * 64 + lane));
    {
      Cx c = {{1.f, 1.f}, {0.f, 0.f}};
      const Cx z8c = {z8.r, -z8.i};
#pragma unroll
      for (int k = 0; k < 16; ++k) { put_vw(vw, 41 + k, c); if (k < 15) c = k ? cmul(c, z8c) : z8c; }
    }
    {
      Cx v[8];
      v[0] = Cx{{bflo(p_cr) * sc, bfhi(p_cr) * sc}, {bflo(p_ci) * sc, bfhi(p_ci) * sc}};
#pragma unroll
      for (int r = 1; r < 8; ++r) v[r] = cmul(v[r - 1], z1);
#pragma unroll
      for (int r = 0; r < 8; ++r) { put_vw(vw, r, v[r]); v[r] = cmul(v[r], z128); put_vw(vw, 16 + r, v[r]); }
#pragma unroll
      for (int r = 0; r < 8; r += 2) {
        v[r] = cmul(v[r], z128); put_vw(vw, 8 + r / 2, v[r]);
        v[r] = cmul(v[r], z128); put_vw(vw, 24 + r / 2, v[r]);
      }
#pragma unroll
      for (int r = 0; r < 8; r += 4) {
        v[r] = cmul(v[r], z128); put_vw(vw, 12 + r / 4, v[r]);
        v[r] = cmul(v[r], z128); put_vw(vw, 28 + r / 4, v[r]);
        v[r] = cmul(v[r], z128); put_vw(vw, 14 + r / 4, v[r]);
        v[r] = cmul(v[r], z128); put_vw(vw, 30 + r / 4, v[r]);
      }
#pragma unroll
      for (int r = 0; r < 9; ++r) { v[0] = cmul(v[0], z128); put_vw(vw, 32 + r, v[0]); }
    }
    VMCNT(0);
    BARRIER();
    signal_global(ws, C_A, w, lane, b);
    STAMP(2);
    {
      f32x4 k0, k2;
      LAS char* pa = opq(sm + L_VW + ch * VW_CH + i16 * VW_ROW + g4 * 256);
      LAS char* p0 = opq(pa + (w & 1) * 16 * VW_ROW);
#pragma unroll
      for (int kk = 0; kk < 16; ++kk) {
        const v4u fw = *(LAS v4u*)(pa + 41 * VW_ROW + kk * 16), f0 = *(LAS v4u*)(p0 + kk * 16), f2 = *(LAS v4u*)(pa + 32 * VW_ROW + kk * 16);
        if (kk == 0) { MF16_0(k0, f0, fw); MF16_0(k2, f2, fw); } else { MF16(k0, f0, fw); MF16(k2, f2, fw); }
      }
      asm volatile("s_nop 7" : "+v"(k0), "+v"(k2));
      LAS char* tp = sm + L_TP + ch * TP_CH;
#pragma unroll
      for (int r = 0; r < 4; ++r) {
        const int row = 4 * g4 + r;
        const int base = row < 8 ? row : (row < 12 ? 256 + 2 * (row - 8) : (row < 14 ? 512 + 4 * (row - 12) : 768 + 4 * (row - 14)));
        *(LAS u16*)(tp + 2 * (base + 128 * (w & 1) + 8 * i16)) = (u16)pkh(k0[r], k0[r]);
        const int p2 = 1024 + 128 * row + 8 * i16;
        *(LAS u16*)(tp + 2 * (p2 < 2052 ? p2 : 2052)) = (u16)pkh(k2[r], k2[r]);
      }
      if (lane == 0) *(LAS u16*)(tp + 2 * 2054) = 0;
    }
    BARRIER();
#pragma unroll
    for (int c2 = 0; c2 < 2; ++c2) {
      unsigned e[11], A[6], B[5];
#pragma unroll
      for (int k = 0; k < 11; ++k) e[k] = *(LAS u16*)(sm + L_TP + c2 * TP_CH + goff[k]);
#pragma unroll
      for (int k = 0; k < 5; ++k) { A[k] = e[2 * k] | (e[2 * k + 1] << 16); B[k] = e[2 * k + 1] | (e[2 * k + 2] << 16); }
      LAS char* kc = sm + L_KC + c2 * KC_CH + 16 * t;
      *(LAS v4u*)(kc) = v4u{A[0], A[1], A[2], A[3]};
      *(LAS v4u*)(kc + KC_COPY) = v4u{B[0], B[1], B[2], B[3]};
      *(LAS v4u*)(kc + 2 * KC_COPY) = v4u{A[1], A[2], A[3], A[4]};
      *(LAS v4u*)(kc + 3 * KC_COPY) = v4u{B[1], B[2], B[3], B[4]};
    }
    if (t < 16) {
      const unsigned k00 = *(LAS u16*)(sm + L_TP + (t >> 3) * TP_CH);
      *(LAS v4u*)(sm + L_KC + (t >> 3) * KC_CH + ((t >> 1) & 3) * KC_COPY + 4096 + (t & 1) * 16) = v4u{(t & 7) == 0 ? k00 : 0u, 0u, 0u, 0u};
    }
    STAMP(3);
    wait_global(ws, C_A, w, lane, b);
    BARRIER();
    if (t < 64) *(LAS v4u*)(sm + L_U + (t >> 5) * U_CH + (t & 31) * 16) = v4u{0u, 0u, 0u, 0u};
    STAMP(4);
  }

  const int t0 = t, w0 = w, b0 = b;
  f32x16 csg, csu;
  v4u wd1[20];

#pragma unroll 1
  for (int layer = 0;; ++layer) {
    const int t = opq(t0), lane = t & 63, w = opqs(w0), b = opqs(b0);
    const int xcd = b & 7, jj = b >> 3, ch = w >> 1;
    const int q4 = (lane & 15) >> 2, p4 = lane & 3, grp = (lane >> 4) & 1, hh = lane >> 5, i16 = lane & 15, g4 = lane >> 4;
    const int pan = 2 * xcd + jj / 14, ft = jj % 14, lt = 4 * xcd + jj / 7, dt = jj % 7;
    const int xin = WS_X0 + 2 * layer * WS_XSTR, xmid = xin + WS_XSTR, stin = layer ? WS_ST2 : WS_ST0, hbuf = WS_H + layer * WS_HSTR;
    {
      const int o = t & 127, rvoff = opq(lane * 16), rsoff = WS_WGU + (4 * ft + w) * 57344;
      LAS char* abase = opq(sm + L_KC + ch * KC_CH + ((-i16) & 3) * KC_COPY + 2 * (2032 + 8 * g4 - 4 * ((i16 + 3) >> 2)) - 3584 - 192 - 256 * (w & 1));
      LAS char* bbase = opq(sm + L_U + ch * U_CH + 2 * (240 + 16 * i16 + 8 * g4) - 192 - 256 * (w & 1));
      LAS char* pw = opq(sm + L_PS + ((w & 1) * 2 + ch) * 4096 + 64 * i16 + 16 * (g4 ^ ((i16 >> 1) & 3)));
      const int pcol = (o >> 1) & 15;
      LAS char* pr0 = opq(sm + L_PS + ch * 4096 + (o >> 5) * 1024 + 64 * pcol + 16 * ((2 * (o & 1)) ^ ((pcol >> 1) & 3)));
      LAS char* pr1 = opq(sm + L_PS + ch * 4096 + (o >> 5) * 1024 + 64 * pcol + 16 * ((2 * (o & 1) + 1) ^ ((pcol >> 1) & 3)));
      const int xvoff = (o >> 2) * SLAB + (2 * b + ch) * 64 + (o & 3) * 16;
      f32x4 acc[8];
      float base[8];
      auto pro = [&](int half) AINL {
        const v4u xv = ld16(rs, xvoff, xin + half * 32 * SLAB);
        v4u sv[8];
#pragma unroll
        for (int j = 0; j < 8; ++j) sv[j] = ld16(rs, o * 16, stin + (j * 256 + 128 * half) * 16);
        float u[8];
#pragma unroll
        for (int j = 0; j < 8; ++j) {
          const float s = (float)__builtin_bit_cast(double, v2u{sv[j].x, sv[j].y}), q = (float)__builtin_bit_cast(double, v2u{sv[j].z, sv[j].w});
          const float m = s * (1.f / 448.f), var = fmaxf(q * (1.f / 448.f) - m * m, 0.f), r = __builtin_amdgcn_rsqf(var + 1e-5f);
          const float x = (j & 1) ? bfhi(xv[j >> 1]) : bflo(xv[j >> 1]);
          u[j] = (x - m) * r; base[j] = x + u[j] * dskip;
        }
        *(LAS v4u*)(sm + L_U + ch * U_CH + 512 + half * 2048 + o * 16) = v4u{pkh(u[0], u[1]), pkh(u[2], u[3]), pkh(u[4], u[5]), pkh(u[6], u[7])};
        BARRIER();
      };
      auto epi = [&](int half) AINL {
        asm volatile("s_nop 7" ::: "memory");
#pragma unroll
        for (int c = 0; c < 4; ++c) *(LAS f32x4*)(pw + c * 1024) = acc[4 * half + c];
        BARRIER();
        const f32x4 y0 = *(LAS f32x4*)pr0 + *(LAS f32x4*)(pr0 + 8192), y1 = *(LAS f32x4*)pr1 + *(LAS f32x4*)(pr1 + 8192);
        v4u ov;
#pragma unroll
        for (int e = 0; e < 2; ++e) {
          ov[e] = pkbf(base[2 * e] + y0[2 * e] * kscale, base[2 * e + 1] + y0[2 * e + 1] * kscale);
          ov[2 + e] = pkbf(base[4 + 2 * e] + y1[2 * e] * kscale, base[5 + 2 * e] + y1[2 * e + 1] * kscale);
        }
        st16(ov, rs, xvoff, xmid + half * 32 * SLAB);
      };
      if (layer) { wait_global(ws, C_XA, w, lane, b); BARRIER(); }
      STAMP(5 + 13 * layer);
      pro(0);
      conv_part<0, 4, 0, 4, true, 0, 10>(acc, abase, bbase, rs, rvoff, rsoff);
      epi(0);
      conv_part<4, 8, 0, 4, true, 10, 16>(acc, abase, bbase, rs, rvoff, rsoff);
      VMCNT(16);
      if (layer) wait_global(ws, C_XB, w, lane, b);
      BARRIER();
      signal_global(ws, C_CA0 + 2 * layer, w, lane, b);
      STAMP(6 + 13 * layer);
      pro(1);
      conv_part<4, 8, 4, 8, false, 26, 10>(acc, abase, bbase, rs, rvoff, rsoff);
      epi(1);
      SFOR(i, 36, 56) ld_slot<i, i>(rs, rvoff, rsoff); SEND
      VMCNT(20);
      BARRIER();
      signal_global(ws, C_CB0 + 2 * layer, w, lane, b);
      STAMP(7 + 13 * layer);
    }
    const int dvoff = opq(lane * 16), dsoff = WS_WD + (dt * 4 + w) * 57344;
    {
      if (layer == 0) {
        const unsigned one2 = opq(0x3f803f80);
        v4u ones = {one2, one2, one2, one2};
        asm volatile("s_nop 1" : "+v"(ones));
        SFOR(i, 0, 28)
          if constexpr (i == 18) VMCNT(0);
          mf_av<i == 0, 2 * i>(csg, ones); mf_av<i == 0, 2 * i + 1>(csu, ones);
        SEND
      }
      const int svoff = opq(lane * 16 + w * 7168);
      auto dma_slab = [&](int tt) AINL {
        const int slab = (tt >> 1) * 32 + 2 * pan + (tt & 1), buf = tt == 3 ? 0 : tt;
#pragma unroll
        for (int k = 0; k < 7; ++k) dma16(rs, smb + L_BUF + buf * SLAB + w * 7168 + k * 1024, svoff, xmid + slab * SLAB + k * 1024);
      };
      wait_global(ws, C_CA0 + 2 * layer, w, lane, b);
      BARRIER();
      STAMP(8 + 13 * layer);
      dma_slab(0); dma_slab(1);
      VMCNT(7);
      BARRIER();
      STAMP(9 + 13 * layer);
      f32x16 ag[2], au[2];
      const int baddr = (8 * hh + q4) * 64 + (4 * grp + p4) * 8;
      const int hvoff = opq(lane * 16 + (4 * ft + w) * 2048);
      float mean, nrl, sd, hv[16];
      auto stats_rd = [&](int tt) AINL {
        const f32x4 a = *(LAS f32x4*)(sm + L_SM + (tt & 1) * 1024 + (lane & 31) * 16), c = *(LAS f32x4*)(sm + L_SM + (tt & 1) * 1024 + 512 + (lane & 31) * 16);
        mean = ((a[0] + a[1]) + (a[2] + a[3])) * (1.f / 448.f);
        const float var = fmaxf(((c[0] + c[1]) + (c[2] + c[3])) * (1.f / 448.f) - mean * mean, 0.f) + 1e-5f;
        const float r = __builtin_amdgcn_rsqf(var);
        nrl = -1.44269504f * r; sd = var * r;
      };
      auto epi_elem = [&](int tt, int r) AINL {
        float tg = ag[tt & 1][r], tu = au[tt & 1][r];
        PIN(tg); PIN(tu);
        const float e = __builtin_amdgcn_exp2f((tg - mean * csg[r]) * nrl);
        hv[r] = (tu - mean * csu[r]) * __builtin_amdgcn_rcpf(e * sd + sd);
        PIN(hv[r]);
      };
      auto epi_store = [&](int tt, int s) AINL {
        const int slab = (tt >> 1) * 32 + 2 * pan + (tt & 1);
        st16(v4u{pkbf(hv[8 * s], hv[8 * s + 1]), pkbf(hv[8 * s + 2], hv[8 * s + 3]), pkbf(hv[8 * s + 4], hv[8 * s + 5]), pkbf(hv[8 * s + 6], hv[8 * s + 7])},
             rs, hvoff + s * 1024, hbuf + slab * 114688);
      };
      SFOR(tt, 0, 4)
        constexpr int buf = tt == 3 ? 0 : tt;
        LAS char* sb[4];
#pragma unroll
        for (int gi = 0; gi < 4; ++gi) sb[gi] = opq(sm + L_BUF + buf * SLAB + baddr + ((gi + w) & 3) * 7168);
        v4u bf[3];
        auto rdb = [&](int i) AINL { LAS char* p = sb[i / 7] + (i % 7) * 1024; bf[i % 3] = cat(trd(p), trd(p + 256)); };
        rdb(0); rdb(1);
        if (tt > 0) stats_rd(tt - 1);
        if (tt == 1) { dma_slab(2); dma_slab(3); }
        if (tt == 3) {
#pragma unroll
          for (int hf = 0; hf < 2; ++hf) dma16(rs, smb + L_RES + (w * 2 + hf) * 1024, dvoff, xmid + (32 * hf + lt) * SLAB + (64 * dt + 16 * w) * 64);
        }
        float ssum = 0.f, ssq = 0.f;
        SFOR(i, 0, 28)
          if (i + 2 < 28) rdb(i + 2);
          mf_av<i == 0, 2 * i>(ag[tt & 1], bf[i % 3]); mf_av<i == 0, 2 * i + 1>(au[tt & 1], bf[i % 3]);
          if (i < 7) {
#pragma unroll
            for (int e = 0; e < 4; ++e) {
              const unsigned xu = bf[i % 3][e];
              const bf16x2 xx = __builtin_bit_cast(bf16x2, xu);
              ssum = __builtin_amdgcn_fdot2_f32_bf16(xx, bf16x2{(__bf16)1.f, (__bf16)1.f}, ssum, false);
              ssq = __builtin_amdgcn_fdot2_f32_bf16(xx, xx, ssq, false);
            }
            PIN(ssum); PIN(ssq);
          }
          if (i == 7) {
            const auto sw = __builtin_amdgcn_permlane32_swap(__float_as_uint(ssum), __float_as_uint(ssq), false, false);
            *(LAS float*)(sm + L_SM + (tt & 1) * 1024 + lane * 16 + w * 4) = __uint_as_float(sw[0]) + __uint_as_float(sw[1]);
          }
          if constexpr (tt > 0 && i >= 7 && i < 23) {
            epi_elem(tt - 1, i - 7);
            if (i == 14 || i == 22) epi_store(tt - 1, i == 22);
          }
          if constexpr (tt < 3 && (i % 3) == 0 && tt * 10 + i / 3 < 28) {
            constexpr int k = tt * 10 + i / 3;
            if constexpr (k < 20) wd1[k] = ld16(rs, dvoff + (k & 3) * 1024, dsoff + (k >> 2) * 4096);
            else ld_slot<36 + k, k>(rs, dvoff, dsoff);
          }
          if constexpr (tt == 3) ld_slot<2 * i, 28 + i>(rs, dvoff, dsoff);
        SEND
        if (tt == 0) { VMCNT(10); wait_global(ws, C_CB0 + 2 * layer, w, lane, b); }
        if (tt == 1) VMCNT(19);
        if (tt == 2) { VMCNT(0); SFOR(k, 0, 20) PIN(wd1[k]); SEND }
        if (tt == 3) wait_local(ws, layer * 32 + pan, w, lane);
        BARRIER();
        if (tt == 2) signal_local(ws, layer * 32 + pan, w, lane);
        STAMP(10 + tt + 13 * layer);
      SEND
      const int gsoff = hbuf + lt * 114688 + w * 28672;
      stats_rd(3);
      SFOR(r, 0, 16)
        epi_elem(3, r);
        if (r == 7 || r == 15) epi_store(3, r == 15);
        if constexpr (r < 8) ld_slot<2 * r + 1, r>(rs, dvoff, gsoff);
      SEND
      SFOR(k, 8, 28) ld_slot<2 * k + 1, k>(rs, dvoff, gsoff); SEND
      VMCNT(20);
      BARRIER();
      signal_local(ws, layer * 32 + 16 + pan, w, lane);
      STAMP(14 + 13 * layer);
    }
    if (layer) break;
    glu_down<false, 0>(wd1, sm, ws, out, rs, t0, w0, b0);
    STAMP(15);
    glu_down<false, 1>(wd1, sm, ws, out, rs, t0, w0, b0);
    VMCNT(0);
    BARRIER();
    signal_global(ws, C_XA, w, lane, b);
    signal_global(ws, C_XB, w, lane, b);
    STAMP(16);
  }
  glu_down<true, 0>(wd1, sm, ws, out, rs, t0, w0, b0);
  STAMP(28);
  glu_down<true, 1>(wd1, sm, ws, out, rs, t0, w0, b0);
  STAMP(29); RSTAMP(1);
}

extern "C" void kernel_launch(void* const* d_in, const int* in_sizes, int n_in, void* d_out, int out_size, void* d_ws,
                              size_t ws_size, hipStream_t stream) {
  (void)hipMemsetAsync(d_ws, 0, WS_ZERO_BYTES, stream);
  k_fused<<<NB, 256, 0, stream>>>((const u16*)d_in[0], (const u16*)d_in[1], (const u16*)d_in[2], (const u16*)d_in[3],
                                  (const u16*)d_in[4], (const u16*)d_in[5], (const u16*)d_in[6], (const u16*)d_in[8],
                                  (const u16*)d_in[9], (const u16*)d_in[10], (u16*)d_out, (char*)d_ws);
}
```

```cpp
#include <hip/hip_runtime.h>
#include <hip/hip_bf16.h>
#include <stdint.h>
#define NEXTRA 0
typedef unsigned short u16;
constexpr int L=2048, D=448, N=256, F=1792;
__device__ __forceinline__ float b2f(u16 x){ return __uint_as_float(((unsigned)x)<<16); }
__device__ __forceinline__ u16 f2b(float f){ unsigned u=__float_as_uint(f); u += 0x7FFF + ((u>>16)&1); return (u16)(u>>16); }
__device__ __forceinline__ float rb(float f){ return b2f(f2b(f)); }

__global__ void k_kgen(const u16* lar,const u16* aim,const u16* cr,const u16* ci,const u16* ldt,float* K){
  int d=blockIdx.x;
  __shared__ float sar[N],sai[N],scr[N],sci[N];
  float dt=rb(expf(b2f(ldt[d])));
  for(int n=threadIdx.x;n<N;n+=blockDim.x){
    sar[n]=-rb(expf(b2f(lar[d*N+n])))*dt; sai[n]=b2f(aim[d*N+n])*dt;
    scr[n]=b2f(cr[d*N+n])*dt; sci[n]=b2f(ci[d*N+n])*dt;
  }
  __syncthreads();
  for(int l=threadIdx.x;l<L;l+=blockDim.x){
    float p=rb((float)l); float s=0;
    for(int n=0;n<N;n++){ float e=__expf(sar[n]*p); float sn,cs; __sincosf(sai[n]*p,&sn,&cs); s+=e*(scr[n]*cs-sci[n]*sn); }
    K[d*L+l]=2.f*s;
  }
}
__global__ void k_ln(const u16* x,u16* u){
  int l=blockIdx.x; __shared__ float red[64*8];
  float s=0; for(int d=threadIdx.x;d<D;d+=64) s+=b2f(x[l*D+d]);
  for(int o=32;o;o>>=1) s+=__shfl_xor(s,o);
  float m=s/D; float v=0; for(int d=threadIdx.x;d<D;d+=64){ float t=b2f(x[l*D+d])-m; v+=t*t; }
  for(int o=32;o;o>>=1) v+=__shfl_xor(v,o);
  v=rb(v/D); m=rb(m); float sd=rb(sqrtf(rb(v+1e-5f)));
  for(int d=threadIdx.x;d<D;d+=64) u[l*D+d]=f2b(rb(b2f(x[l*D+d])-m)/sd);
}
__global__ void k_conv(const u16* x,const u16* u,const float* K,const u16* dsk,u16* o){
  int d=blockIdx.x; __shared__ float su[L], sk[L];
  for(int l=threadIdx.x;l<L;l+=blockDim.x){ su[l]=b2f(u[l*D+d]); sk[l]=K[d*L+l]; }
  __syncthreads();
  float ds=b2f(dsk[d]);
  for(int l=threadIdx.x;l<L;l+=blockDim.x){
    float s=0; for(int j=0;j<=l;j++) s+=sk[l-j]*su[j];
    float y=rb(rb(s)+rb(su[l]*ds));
    o[l*D+d]=f2b(b2f(x[l*D+d])+y);
  }
}
__global__ void k_g1(const u16* u,const u16* wg,const u16* wu,u16* h){
  int l0=blockIdx.y*8; int f=blockIdx.x*256+threadIdx.x; __shared__ float su[8][D];
  for(int i=threadIdx.x;i<8*D;i+=256) su[i/D][i%D]=b2f(u[l0*D+i]);
  __syncthreads();
  float g[8]={0},uu[8]={0};
  for(int d=0;d<D;d++){ float a=b2f(wg[d*F+f]), b=b2f(wu[d*F+f]);
    for(int r=0;r<8;r++){ g[r]+=su[r][d]*a; uu[r]+=su[r][d]*b; } }
  for(int r=0;r<8;r++){ float gg=rb(g[r]); float sg=rb(1.f/(1.f+__expf(-gg))); h[(l0+r)*F+f]=f2b(sg*rb(uu[r])); }
}
__global__ void k_g2(const u16* x,const u16* h,const u16* wd,u16* o){
  int l0=blockIdx.x*4; int d=threadIdx.x; __shared__ float sh[4][F];
  for(int i=threadIdx.x;i<4*F;i+=D) sh[i/F][i%F]=b2f(h[l0*F+i]);
  __syncthreads();
  float a[4]={0};
  for(int f=0;f<F;f++){ float w=b2f(wd[f*D+d]); for(int r=0;r<4;r++) a[r]+=sh[r][f]*w; }
  for(int r=0;r<4;r++) o[(l0+r)*D+d]=f2b(b2f(x[(l0+r)*D+d])+rb(a[r]));
}
__global__ void k_stamp(unsigned long long* p){ p[0]=__builtin_amdgcn_s_memrealtime(); }
__global__ void k_hold(unsigned long long* p){ unsigned long long t0=p[0]; while(__builtin_amdgcn_s_memrealtime()-t0 < 150000ull) __builtin_amdgcn_s_sleep(1); }
template<int LDS> __global__ void __launch_bounds__(256,1) k_stamp_big(unsigned long long* p){ __shared__ char sm[LDS]; asm volatile("":::"a0","a255","v250"); if(blockIdx.x==0 && threadIdx.x==0) p[0]=__builtin_amdgcn_s_memrealtime(); if(p==nullptr) sm[threadIdx.x]=1, p[1]=sm[3]; }
template<int LDS> __global__ void __launch_bounds__(256,1) k_hold_big(unsigned long long* p){ __shared__ char sm[LDS]; asm volatile("":::"a0","a255","v250"); unsigned long long t0=p[0]; while(__builtin_amdgcn_s_memrealtime()-t0 < 150000ull) __builtin_amdgcn_s_sleep(1); if(p==nullptr) sm[threadIdx.x]=1, p[1]=sm[3]; }
__global__ void k_empty(int* p){ if(p==nullptr) return; }
extern "C" void kernel_launch(void* const* d_in, const int* in_sizes, int n_in, void* d_out, int out_size, void* d_ws, size_t ws_size, hipStream_t stream){
  const u16* x0=(const u16*)d_in[0];
  char* ws=(char*)d_ws;
  float* K=(float*)ws; u16* u=(u16*)(ws+(4<<20)); u16* x1=(u16*)(ws+(8<<20)); u16* x2=(u16*)(ws+(12<<20)); u16* h=(u16*)(ws+(16<<20));
  k_stamp<<<1,64,0,stream>>>((unsigned long long*)(ws+(64<<20)));
  k_kgen<<<D,256,0,stream>>>((const u16*)d_in[1],(const u16*)d_in[2],(const u16*)d_in[3],(const u16*)d_in[4],(const u16*)d_in[5],K);
  const u16* xin=x0;
  for(int it=0;it<2;it++){
    k_ln<<<L,64,0,stream>>>(xin,u);
    k_conv<<<D,256,0,stream>>>(xin,u,K,(const u16*)d_in[6],x1);
    k_ln<<<L,64,0,stream>>>(x1,u);
    k_g1<<<dim3(F/256,L/8),256,0,stream>>>(u,(const u16*)d_in[8],(const u16*)d_in[9],h);
    u16* o = it==0? x2 : (u16*)d_out;
    k_g2<<<L/4,D,0,stream>>>(x1,h,(const u16*)d_in[10],o);
    xin=x2;
  }
  k_hold<<<1,64,0,stream>>>((unsigned long long*)(ws+(64<<20)));
  for(int i=0;i<NEXTRA;i++) k_empty<<<256,256,0,stream>>>((int*)d_ws);
}
```

```cpp
#include <hip/hip_runtime.h>
#include <stdint.h>
#include <type_traits>

typedef unsigned short u16;
typedef unsigned v4u __attribute__((ext_vector_type(4)));
typedef unsigned v2u __attribute__((ext_vector_type(2)));
typedef float f32x2 __attribute__((ext_vector_type(2)));
typedef float f32x4 __attribute__((ext_vector_type(4)));
typedef float f32x16 __attribute__((ext_vector_type(16)));
typedef short s16x4 __attribute__((ext_vector_type(4)));
typedef __bf16 bf16x2 __attribute__((ext_vector_type(2)));
typedef _Float16 f16x2 __attribute__((ext_vector_type(2)));
typedef __amdgpu_buffer_rsrc_t rsrc_t;
#define LAS __attribute__((address_space(3)))
#define GAS __attribute__((address_space(1)))
#define DI __device__ __forceinline__
#define AINL __attribute__((always_inline))

#define CODE_PF 1
#define PSLP "6"
#define HSLP 20
#define HDLY 1
#define HDLY2 20
#define PCA0 98
#define V1 1
#define V2 0
#define CSDLY 0
#define PREH 15
#define MAP47 1
constexpr int NB = 224, NH = 32;
constexpr int SLAB = 448 * 64;
constexpr int GSL = SLAB + 256;
constexpr int WS_CTL = 0;
constexpr int WS_LOC = 8 * 16384;
constexpr int WS_EZ = 9 * 16384;
constexpr int WS_ST2 = 10 * 16384;
constexpr int WS_ST1 = 12 * 16384;
constexpr int WS_ST0 = 14 * 16384;
constexpr int WS_CS = 16 * 16384;
constexpr int WS_DBG = 20 * 16384;
constexpr int WS_X0 = 1 << 20, WS_XSTR = 2 << 20;
constexpr int WS_H = 10 << 20, WS_HSTR = 8 << 20;
constexpr int WS_WGU = 26 << 20;
constexpr int WS_WD = 30 << 20;
constexpr int WS_BYTES = 32 << 20;
enum { C_A = 0, C_W = 1, C_CA0 = 2, C_CB0 = 3, C_CA1 = 4, C_CB1 = 5, C_XA = 6, C_XB = 7 };
constexpr int L_KC = 0, KC_COPY = 4160, KC_CH = 4 * KC_COPY;
constexpr int L_U = 2 * KC_CH, U_CH = 4608;
constexpr int L_SM = L_U + 2 * U_CH;
constexpr int L_BUF = L_SM + 2048;
constexpr int L_PS = L_BUF + 2 * SLAB;
constexpr int L_RES = L_BUF;
constexpr int G2S = 9216, L_G2 = L_BUF + 8192;
constexpr int LDS_BYTES = L_BUF + 4 * SLAB;
constexpr int L_VW = 0, VW_ROW = 1040, VW_CH = 57 * VW_ROW;
constexpr int L_XT = 2 * VW_CH, L_WR = L_XT + 8192, L_TP = L_WR + 24576, TP_CH = 1344;
static_assert(LDS_BYTES <= 163840 && L_TP + 2 * TP_CH + 16 <= LDS_BYTES && L_G2 + 8 * G2S <= L_BUF + 3 * SLAB, "lds");
constexpr int NV = 30;

template <class T> DI void PIN(T& x) { asm volatile("" : "+v"(x)); }
#define WAITV(tag, n) asm volatile("s_waitcnt vmcnt(%0) ; NEED " #tag ::"n"(n) : "memory")
#define VMW(n) asm volatile("s_waitcnt vmcnt(%0)" ::"n"(n) : "memory")
#define WAITV0(tag, n, w) asm volatile("s_waitcnt vmcnt(%1)\n\ts_cmp_lg_u32 %2, 0\n\ts_cbranch_scc0 1f\n\ts_waitcnt vmcnt(%0) ; NEED " #tag "\n1:" ::"n"(n), "n"((n) + 1), "s"(w) : "scc", "memory")
#define MARK(tag) asm volatile("; MARK " #tag ::: "memory")
#define BARRIER() asm volatile("s_waitcnt lgkmcnt(0)\n\ts_barrier" ::: "memory")
#define SBAR() asm volatile("s_barrier" ::: "memory")
DI void MF16_0(f32x4& acc, v4u a, v4u b) { asm volatile("v_mfma_f32_16x16x32_f16 %0, %1, %2, 0" : "=&v"(acc) : "v"(a), "v"(b)); }
DI void MF16(f32x4& acc, v4u a, v4u b) { asm volatile("v_mfma_f32_16x16x32_f16 %0, %1, %2, %0" : "+v"(acc) : "v"(a), "v"(b)); }
#define MFOP "v_mfma_f32_32x32x16_bf16 %0, "
DI void MF_VV(f32x16& acc, v4u a, v4u b) { asm volatile(MFOP "%1, %2, %0" : "+v"(acc) : "v"(a), "v"(b)); }
template <bool Z, int S> DI void mf_av(f32x16& acc, v4u b) {
  if (Z) asm volatile(MFOP "a[%c2:%c3], %1, 0" : "=&v"(acc) : "v"(b), "n"(4 * S), "n"(4 * S + 3));
  else asm volatile(MFOP "a[%c2:%c3], %1, %0" : "+v"(acc) : "v"(b), "n"(4 * S), "n"(4 * S + 3));
}
template <bool Z, int S> DI void mf_va(f32x16& acc, v4u a) {
  if (Z) asm volatile(MFOP "%1, a[%c2:%c3], 0" : "=&v"(acc) : "v"(a), "n"(4 * S), "n"(4 * S + 3));
  else asm volatile(MFOP "%1, a[%c2:%c3], %0" : "+v"(acc) : "v"(a), "n"(4 * S), "n"(4 * S + 3));
}
template <int S, int T> DI void mf_aa(f32x16& acc) {
  asm volatile(MFOP "a[%c1:%c2], a[%c3:%c4], %0" : "+v"(acc) : "n"(4 * S), "n"(4 * S + 3), "n"(4 * T), "n"(4 * T + 3));
}
template <int S, int K> DI void ld_slot(rsrc_t r, int voff, int soff) {
  asm volatile("buffer_load_dwordx4 a[%c0:%c1], %2, %3, %4 offen offset:%c5 sc1" ::"n"(4 * S), "n"(4 * S + 3), "v"(voff), "s"(r),
               "s"(soff + (K >> 2) * 4096), "n"((K & 3) * 1024) : "memory");
}
template <int K> DI void ld_reg(v4u& d, rsrc_t r, int voff, int soff) {
  asm volatile("buffer_load_dwordx4 %0, %1, %2, %3 offen offset:%c4 sc1" : "=&v"(d) : "v"(voff), "s"(r), "s"(soff + (K >> 2) * 4096),
               "n"((K & 3) * 1024) : "memory");
}
template <int S, int K> DI void ld_slot_c(rsrc_t r, int voff, int cur) {
  asm volatile("buffer_load_dwordx4 a[%c0:%c1], %2, %3, %4 offen offset:%c5 sc1" ::"n"(4 * S), "n"(4 * S + 3), "v"(voff), "s"(r), "s"(cur), "n"((K & 3) * 1024) : "memory");
}
template <int K> DI void ld_reg_c(v4u& d, rsrc_t r, int voff, int cur) {
  asm volatile("buffer_load_dwordx4 %0, %1, %2, %3 offen offset:%c4 sc1" : "=&v"(d) : "v"(voff), "s"(r), "s"(cur), "n"((K & 3) * 1024) : "memory");
}
DI void adv(int& cur) { asm volatile("s_add_u32 %0, %0, 0x1000" : "+s"(cur)::"scc"); }
DI void v_mul(float& d, float a, float b) { asm volatile("v_mul_f32 %0, %1, %2" : "=&v"(d) : "v"(a), "v"(b)); }
DI void v_exp(float& d) { asm volatile("v_exp_f32 %0, %0" : "+v"(d)); }
DI void v_rcp(float& d) { asm volatile("v_rcp_f32 %0, %0" : "+v"(d)); }
DI void v_fma1(float& d, float s) { asm volatile("v_fma_f32 %0, %0, %1, %1" : "+v"(d) : "v"(s)); }
DI void v_pk(unsigned& d, float a, float b) { asm volatile("v_cvt_pk_bf16_f32 %0, %1, %2" : "=&v"(d) : "v"(a), "v"(b)); }
template <int I, int N, class Fn> DI void sfor(Fn&& f) {
  if constexpr (I < N) { f(std::integral_constant<int, I>{}); sfor<I + 1, N>(f); }
}
#define SFOR(i, lo, hi) sfor<lo, hi>([&](auto i##_) AINL { constexpr int i = decltype(i##_)::value;
#define SEND });

#define RSTAMP0()
#define RSTAMP1()
#define STAMP(i)

DI float bflo(unsigned u) { return __uint_as_float(u << 16); }
DI float bfhi(unsigned u) { return __uint_as_float(u & 0xffff0000u); }
DI unsigned pkbf(float a, float b) { bf16x2 v = {(__bf16)a, (__bf16)b}; return __builtin_bit_cast(unsigned, v); }
DI unsigned pkh(float a, float b) { const f32x2 v = {a, b}; return __builtin_bit_cast(unsigned, __builtin_convertvector(v, f16x2)); }
DI float rbf(float a) { return bflo(pkbf(a, a)); }
DI float dot2(unsigned a, unsigned b, float c) {
  return __builtin_amdgcn_fdot2_f32_bf16(__builtin_bit_cast(bf16x2, a), __builtin_bit_cast(bf16x2, b), c, false);
}
DI v2u trd(LAS char* p) { return __builtin_bit_cast(v2u, __builtin_amdgcn_ds_read_tr16_b64_v4i16((LAS s16x4*)p)); }
DI v4u cat(v2u a, v2u b) { return v4u{a.x, a.y, b.x, b.y}; }
template <int OFF = 0> DI void st16(v4u v, rsrc_t r, int voff, int soff) {
  asm volatile("buffer_store_dwordx4 %0, %1, %2, %3 offen offset:%c4 sc1\n\ts_nop 1" ::"v"(v), "v"(voff), "s"(r), "s"(soff), "n"(OFF) : "memory");
}
DI int opq(int x) { PIN(x); return x; }
DI int opqs(int x) { asm volatile("" : "+s"(x)); return x; }
DI char* opqs(char* p) { asm volatile("" : "+s"(p)); return p; }
DI LAS char* opq(LAS char* p) { unsigned x = (unsigned)(size_t)p; PIN(x); return (LAS char*)(size_t)x; }
DI void dma16(rsrc_t r, unsigned lds, int voff, int soff) {
  asm volatile("s_mov_b32 m0, %0\n\ts_nop 0\n\tbuffer_load_dwordx4 %1, %2, %3 offen sc1 lds" ::"s"(lds), "v"(voff), "s"(r), "s"(soff) : "memory");
}
template <int OFF> DI void dma_first(rsrc_t r, unsigned lds, int voff, int soff) {
  asm volatile("s_add_u32 m0, %0, %c4\n\ts_nop 0\n\tbuffer_load_dwordx4 %1, %2, %3 offen sc1 lds" ::"s"(lds), "v"(voff), "s"(r), "s"(soff), "n"(OFF) : "scc", "memory");
}
template <int K> DI void dma_next(rsrc_t r, int voff, int soff) {
  asm volatile("buffer_load_dwordx4 %0, %1, %2 offen offset:%c3 sc1 lds" ::"v"(voff), "s"(r), "s"(soff), "n"(K * 1024) : "memory");
}
DI void dma16g(const void* g, int voff, unsigned lds) {
  asm volatile("s_mov_b32 m0, %0\n\ts_nop 0\n\tglobal_load_lds_dwordx4 %1, %2" ::"s"(lds), "v"(voff), "s"(g) : "memory");
}
DI void ld_glob(v4u& d, const void* g, int voff) { asm volatile("global_load_dwordx4 %0, %1, %2" : "=&v"(d) : "v"(voff), "s"(g) : "memory"); }
template <int C> DI float dpp_add(float v) {
  return v + __builtin_bit_cast(float, __builtin_amdgcn_update_dpp(0, __builtin_bit_cast(int, v), C, 0xf, 0xf, true));
}
template <int C, int RM> DI unsigned dpp_max(unsigned v) {
  unsigned o = (unsigned)__builtin_amdgcn_update_dpp(0, (int)v, C, RM, 0xf, true); return v > o ? v : o;
}
DI unsigned wave_max63(unsigned v) {
  v = dpp_max<0x111, 0xf>(v); v = dpp_max<0x112, 0xf>(v); v = dpp_max<0x114, 0xf>(v); v = dpp_max<0x118, 0xf>(v);
  v = dpp_max<0x142, 0xa>(v); v = dpp_max<0x143, 0xc>(v); return v;
}
DI float swap32_add(float a, float b) {
  const auto s = __builtin_amdgcn_permlane32_swap(__float_as_uint(a), __float_as_uint(b), false, false);
  return __uint_as_float(s[0]) + __uint_as_float(s[1]);
}
DI float swap16_add(float a, float b) {
  const auto s = __builtin_amdgcn_permlane16_swap(__float_as_uint(a), __float_as_uint(b), false, false);
  return __uint_as_float(s[0]) + __uint_as_float(s[1]);
}

DI void add_one(char* base, int voff, int w, unsigned long long mask) {
  asm volatile("s_cmp_lg_u32 %3, 0\n\ts_cbranch_scc1 1f\n\ts_mov_b64 exec, %4\n\tglobal_atomic_add %0, %1, %2\n\ts_mov_b64 exec, -1\n1:"
               ::"v"(voff), "v"(1), "s"(base), "s"(w), "s"(mask) : "scc", "memory");
}
template <int SLP> DI void poll_ge(char* base, int voff, int w, unsigned target) {
  unsigned v, cnt;
  asm volatile("s_cmp_lg_u32 %4, 0\n\ts_cbranch_scc1 2f\n\ts_mov_b32 %1, 0x40000\n1:\n\tglobal_load_dword %0, %2, %3 sc1\n\t"
               "s_waitcnt vmcnt(0)\n\tv_cmp_gt_u32 vcc, %5, %0\n\ts_cbranch_vccz 2f\n\ts_sleep %6\n\ts_sub_u32 %1, %1, 1\n\ts_cbranch_scc0 1b\n2:"
               : "=&v"(v), "=&s"(cnt) : "v"(voff), "s"(base), "s"(w), "s"(target), "n"(SLP) : "vcc", "scc", "memory");
}
DI void poll_issue(unsigned& v, char* base, int voff, int w) {
  asm volatile("s_cmp_lg_u32 %3, 0\n\ts_cbranch_scc1 1f\n\tglobal_load_dword %0, %1, %2 sc1\n1:" : "=&v"(v) : "v"(voff), "s"(base), "s"(w) : "scc", "memory");
}
template <int N> DI void poll_check(unsigned& v, char* base, int voff, unsigned target, int w) {
  unsigned cnt;
  asm volatile("s_cmp_lg_u32 %6, 0\n\ts_cbranch_scc1 2f\n\ts_waitcnt vmcnt(%5)\n\tv_cmp_gt_u32 vcc, %4, %0\n\ts_cbranch_vccz 2f\n\ts_mov_b32 %1, 0x40000\n"
               "1:\n\ts_sleep " PSLP "\n\tglobal_load_dword %0, %2, %3 sc1\n\ts_waitcnt vmcnt(0)\n\tv_cmp_gt_u32 vcc, %4, %0\n\ts_cbranch_vccz 2f\n\t"
               "s_sub_u32 %1, %1, 1\n\ts_cbranch_scc0 1b\n2:\n\ts_barrier"
               : "+v"(v), "=&s"(cnt) : "v"(voff), "s"(base), "s"(target), "n"(N), "s"(w) : "vcc", "scc", "memory");
}
template <int OFF> DI void gran_issue(v2u& v, char* base, int voff) {
  asm volatile("global_load_dwordx2 %0, %1, %2 offset:%c3 sc1" : "=&v"(v) : "v"(voff), "s"(base), "n"(OFF) : "memory");
}
template <int N, int OFF> DI void gran_check(v2u& v, char* base, int voff) {
  unsigned cnt;
  asm volatile("s_waitcnt vmcnt(%4)\n\tv_cmp_eq_u64 vcc, 0, %0\n\ts_cbranch_vccz 2f\n\ts_mov_b32 %1, 0x40000\n"
               "1:\n\ts_sleep 2\n\tglobal_load_dwordx2 %0, %2, %3 offset:%c5 sc1\n\ts_waitcnt vmcnt(0)\n\tv_cmp_eq_u64 vcc, 0, %0\n\ts_cbranch_vccz 2f\n\t"
               "s_sub_u32 %1, %1, 1\n\ts_cbranch_scc0 1b\n2:"
               : "+v"(v), "=&s"(cnt) : "v"(voff), "s"(base), "n"(N), "n"(OFF) : "vcc", "scc", "memory");
}
DI void tag_issue(unsigned long long& v, char* base, int voff) { asm volatile("global_load_dwordx2 %0, %1, %2 sc1" : "=&v"(v) : "v"(voff), "s"(base) : "memory"); }
DI void zero_check(unsigned long long& v, unsigned long long tag, char* base, int voff, int w) {
  unsigned cnt;
  asm volatile("s_cmp_lg_u32 %5, 0\n\ts_cbranch_scc1 2f\n\ts_mov_b32 %1, 0x40000\n1:\n\tv_cmp_ne_u64 vcc, %4, %0\n\ts_cbranch_vccz 2f\n\t"
               "s_sleep 2\n\tglobal_load_dwordx2 %0, %2, %3 sc1\n\ts_waitcnt vmcnt(0)\n\ts_sub_u32 %1, %1, 1\n\ts_cbranch_scc0 1b\n2:"
               : "+v"(v), "=&s"(cnt) : "v"(voff), "s"(base), "v"(tag), "s"(w) : "vcc", "scc", "memory");
}
DI int sig_off(int lane, int b) { return ((lane & 7) * 8 + (b & 7)) * 256; }
DI int pol_off(int lane, int b) { return ((b & 7) * 8 + (lane & 7)) * 256; }
DI char* ctr(char* ws, int c) { return ws + WS_CTL + c * 16384; }
DI int st_idx(int l) { return ((l & 1024) + (l & 3) * 256 + ((l & 1023) >> 2)) * 16; }

struct Cx { f32x2 r, i; };
DI Cx cmul(Cx a, Cx b) { Cx c; c.r = a.r * b.r - a.i * b.i; c.i = a.r * b.i + a.i * b.r; return c; }
DI Cx cpow(f32x2 lr, f32x2 rev, float p) {
  Cx z;
#pragma unroll
  for (int k = 0; k < 2; ++k) {
    const float m = __builtin_amdgcn_exp2f(lr[k] * p), f = __builtin_amdgcn_fractf(rev[k] * p);
    z.r[k] = m * __builtin_amdgcn_cosf(f); z.i[k] = m * __builtin_amdgcn_sinf(f);
  }
  return z;
}
template <int ROW> DI void put_vw(LAS char* p, Cx v) {
  *(LAS v2u*)(p + ROW * VW_ROW) = v2u{pkh(v.r[0], v.r[1]), pkh(v.i[0], v.i[1])};
  asm volatile("" ::: "memory");
}

template <int C0, int C1, int M0, int M1> struct ConvPlan {
  static constexpr int E0 = (C0 - (M1 - 1)) > 0 ? (C0 - (M1 - 1)) : 0, NA = C1 - M0 - E0, NBF = M1 - M0, NRD = 2 * NA + NBF;
  static constexpr int count() { int n = 0; for (int m = M0; m < M1; ++m) for (int c = C0; c < C1; ++c) if (c >= m) ++n; return n; }
  static constexpr int NM = count();
  static constexpr int mc(int k) { int n = 0; for (int m = M0; m < M1; ++m) for (int c = C0; c < C1; ++c) if (c >= m) { if (n == k) return m * 8 + c; ++n; } return 0; }
  static constexpr int FA = C0 - M0 - E0;
  static constexpr int ord(int k) { return k == 0 ? 0 : (k <= 2 * NA ? NBF + (k - 1 + 2 * FA) % (2 * NA) : k - 2 * NA); }
};
template <int C0, int C1, int M0, int M1, bool FRESH> struct ConvPart {
  using P = ConvPlan<C0, C1, M0, M1>;
  v2u a[2][2 * P::NA]; v4u bb[2][P::NBF];
  LAS char *abase, *abase8, *bbase;
  DI void rd(int buf, int rho, int k) {
    if (k < P::NBF) bb[buf][k] = *(LAS v4u*)(bbase - 64 * rho + 192 + (M0 + k) * 512);
    else { const int x = k - P::NBF; a[buf][x] = *(LAS v2u*)(((x & 1) ? abase8 : abase) - 64 * rho + 192 + (7 - P::E0 - (x >> 1)) * 512); }
    asm volatile("" ::: "memory");
  }
  DI void preA() { SFOR(k, 0, 2 * P::NA) rd(0, 0, P::ord(k + 1)); SEND }
  DI void preB() { SFOR(k, 0, P::NBF) rd(0, 0, k); SEND }
  template <class Hook> DI void run(f32x4 (&acc)[8], Hook&& hook) {
    SFOR(n, 0, 4 * P::NM)
      constexpr int rho = n / P::NM, k = n % P::NM, m = P::mc(k) >> 3, c = P::mc(k) & 7, e = c - m - P::E0;
      if (FRESH && rho == 0 && m == M0) MF16_0(acc[c], cat(a[0][2 * e], a[0][2 * e + 1]), bb[0][m - M0]);
      else MF16(acc[c], cat(a[rho & 1][2 * e], a[rho & 1][2 * e + 1]), bb[rho & 1][m - M0]);
      if constexpr (rho < 3) { SFOR(x, k * P::NRD / P::NM, (k + 1) * P::NRD / P::NM) rd((rho + 1) & 1, rho + 1, P::ord(x)); SEND }
      hook(std::integral_constant<int, n>{});
    SEND
  }
};
struct Pro { v4u x, s[4]; };
template <int J> DI void pro_load(Pro& p, rsrc_t rs, int xvoff, int svoff, int xsoff, int ssoff) {
  if constexpr (J < 4) ld_reg<4 * J>(p.s[J], rs, svoff, ssoff);
  else ld_reg<0>(p.x, rs, xvoff, xsoff);
}
DI unsigned nb1(unsigned v) { return (unsigned)__builtin_amdgcn_update_dpp(0, (int)v, 0xb1, 0xf, 0xf, true); }
template <int J> DI void pro0_load(Pro& p, rsrc_t rs, int xvoff, int svoff, int half) {
  if constexpr (J < 2) ld_reg<4 * J>(p.s[J], rs, svoff, WS_ST0 + half * 8192);
  else ld_reg<0>(p.x, rs, xvoff, WS_X0 + half * 32 * GSL);
}
template <bool FINAL, class Hook>
DI void pro_finish(Pro& p, float* base, const float (&dskip)[2], LAS char* udst, bool odd, Hook&& hook) {
  PIN(p.x);
  unsigned xx[2][2];
#pragma unroll
  for (int e = 0; e < 2; ++e) { const unsigned lo = p.x[e], hi = p.x[2 + e], nl = nb1(lo), nh = nb1(hi); xx[0][e] = odd ? nh : lo; xx[1][e] = odd ? hi : nl; }
  float u[8];
  SFOR(j, 0, 4)
    float r, c0;
    if constexpr (FINAL) {
      PIN(p.s[j >> 1]);
      r = __uint_as_float(p.s[j >> 1][2 * (j & 1)]); c0 = __uint_as_float(p.s[j >> 1][2 * (j & 1) + 1]);
    } else {
      PIN(p.s[j]);
      const float s = (float)__builtin_bit_cast(double, v2u{p.s[j].x, p.s[j].y}), q = (float)__builtin_bit_cast(double, v2u{p.s[j].z, p.s[j].w});
      const float m = s * (1.f / 448.f), var = fmaxf(q * (1.f / 448.f) - m * m, 0.f);
      r = __builtin_amdgcn_rsqf(var + 1e-5f); c0 = -m * r;
    }
#pragma unroll
    for (int c = 0; c < 2; ++c) {
      const float x = (j & 1) ? bfhi(xx[c][j >> 1]) : bflo(xx[c][j >> 1]);
      u[4 * c + j] = x * r + c0; base[4 * c + j] = x;
    }
    hook(j_);
  SEND
  *(LAS v2u*)udst = v2u{pkh(u[0], u[1]), pkh(u[2], u[3])};
  *(LAS v2u*)(udst + U_CH) = v2u{pkh(u[4], u[5]), pkh(u[6], u[7])};
  asm volatile("" ::: "memory");
#pragma unroll
  for (int j = 0; j < 8; ++j) { base[j] += u[j] * dskip[j >> 2]; PIN(base[j]); }
}
DI int x_voff(int t, int b) { return (t >> 3) * GSL + b * 128 + (t & 1) * 64 + ((t >> 1) & 3) * 16; }

DI void helper(const u16* x0, const u16* wg, const u16* wu, char* ws, rsrc_t rs, LAS char* sm, unsigned smb) {
  const int t = threadIdx.x, lane = t & 63, w = __builtin_amdgcn_readfirstlane(t >> 6), b = blockIdx.x, hb = b - NB;
  unsigned long long tag, zv;
  tag_issue(tag, ws, WS_EZ);
  if (t < 128) st16(v4u{0u, 0u, 0u, 0u}, rs, hb * 2048 + t * 16, WS_ST2);
  else if (t < 142) __builtin_amdgcn_raw_buffer_store_b32(0u, rs, (hb * 14 + t - 128) * 256, WS_CTL + 2 * 16384, 16);
  v4u xr[14];
  SFOR(k, 0, 14)
    ld_glob(xr[k], (const char*)x0 + hb * 57344 + (k / 7) * 28672 + (k % 7) * 128, (t >> 3) * 896 + (t & 7) * 16);
  SEND
  asm volatile("s_waitcnt vmcnt(14)" : "+v"(tag)::"memory");
  tag += 1;
  const unsigned one2 = opq(0x3f803f80), onelo = opq(0x00003f80), onehi = opq(0x3f800000);
  asm volatile("s_waitcnt vmcnt(0)" ::: "memory");
  tag_issue(zv, ws, WS_EZ + (1 + (lane & 7)) * 256);
#pragma unroll
  for (int r = 0; r < 2; ++r) {
    float s = 0.f, q = 0.f;
#pragma unroll
    for (int k = 0; k < 7; ++k) {
      PIN(xr[7 * r + k]);
#pragma unroll
      for (int e = 0; e < 4; ++e) { const unsigned u = xr[7 * r + k][e]; s = dot2(u, one2, s); q = dot2(u, u, q); }
    }
    s = dpp_add<0xb1>(s); q = dpp_add<0xb1>(q); s = dpp_add<0x4e>(s); q = dpp_add<0x4e>(q); s = dpp_add<0x141>(s); q = dpp_add<0x141>(q);
    const float m = s * (1.f / 448.f), rstd = __builtin_amdgcn_rsqf(fmaxf(q * (1.f / 448.f) - m * m, 0.f) + 1e-5f);
    const int l = 64 * hb + 32 * r + (t >> 3);
    if ((t & 7) == 0)
      __builtin_amdgcn_raw_buffer_store_b64(v2u{__float_as_uint(rstd), __float_as_uint(-m * rstd)}, rs,
                                            (l >> 10) * 8192 + ((l >> 1) & 1) * 4096 + ((l & 1023) >> 2) * 16 + (l & 1) * 8, WS_ST0, 16);
  }
  asm volatile("s_waitcnt vmcnt(0)" : "+v"(zv)::"memory");
  zero_check(zv, tag, ws, WS_EZ + (1 + (lane & 7)) * 256, w);
  BARRIER();
  add_one(ctr(ws, C_A), sig_off(lane, b), w, 0xffull);
  for (int i = 0; i < CSDLY; ++i) __builtin_amdgcn_s_sleep(16);
  if (hb < 28) {
    const char* wsrc = (const char*)(hb < 14 ? wg : wu) + (hb % 14) * 256 + (t >> 4) * 3584 + (t & 15) * 16;
    float c[8];
#pragma unroll
    for (int e = 0; e < 8; ++e) c[e] = 0.f;
#pragma unroll 1
    for (int k0 = 0; k0 < 28; k0 += 14) {
      v4u wr[14];
#pragma unroll
      for (int k = 0; k < 14; ++k) wr[k] = *(const v4u*)(wsrc + 16 * (k0 + k) * 3584);
#pragma unroll
      for (int k = 0; k < 14; ++k)
#pragma unroll
        for (int e = 0; e < 4; ++e) { const unsigned u = wr[k][e]; c[2 * e] = dot2(u, onelo, c[2 * e]); c[2 * e + 1] = dot2(u, onehi, c[2 * e + 1]); }
    }
#pragma unroll
    for (int e = 0; e < 4; ++e) c[e] = swap32_add(c[e], c[4 + e]);
#pragma unroll
    for (int e = 0; e < 2; ++e) c[e] = swap16_add(c[e], c[2 + e]);
    *(LAS f32x2*)(sm + w * 512 + ((lane & 15) * 8 + (lane >> 5) * 4 + ((lane >> 4) & 1) * 2) * 4) = f32x2{c[0], c[1]};
    BARRIER();
    if (t < 32) {
      const f32x4 v = (*(LAS f32x4*)(sm + t * 16) + *(LAS f32x4*)(sm + 512 + t * 16)) + (*(LAS f32x4*)(sm + 1024 + t * 16) + *(LAS f32x4*)(sm + 1536 + t * 16));
      st16(__builtin_bit_cast(v4u, v), rs, t * 16, WS_CS + hb * 512);
    }
  }
  asm volatile("s_waitcnt vmcnt(0)" ::: "memory");
  poll_ge<HSLP>(ctr(ws, C_A), pol_off(lane, b), w, (NB + NH) / 8);
  BARRIER();
  if (hb == 0 && t == 0) __builtin_amdgcn_raw_buffer_store_b64(__builtin_bit_cast(v2u, tag), rs, 0, WS_EZ, 16);
  add_one(ctr(ws, C_CA0), sig_off(lane, b), w, 0xffull);
  const int baddr = (8 * (lane >> 5) + ((lane & 15) >> 2)) * 64 + (4 * ((lane >> 4) & 1) + (lane & 3)) * 8;
#pragma unroll 1
  for (int ph = 0; ph < 4; ++ph) {
    for (int i = 0; i < (ph == 2 ? HDLY2 : HDLY); ++i) __builtin_amdgcn_s_sleep(16);
    poll_ge<HSLP>(ctr(ws, C_CA0 + ph), pol_off(lane, b), w, ph ? NB / 8 : (NB + NH) / 8);
    BARRIER();
    const int slab = (ph & 1) * 32 + hb;
#pragma unroll
    for (int k = 0; k < 7; ++k) dma16(rs, smb + 4096 + w * 7168 + k * 1024, lane * 16, WS_X0 + (1 + (ph & 2)) * WS_XSTR + slab * GSL + w * 7168 + k * 1024);
    float s = 0.f, q = 0.f;
    SFOR(k, 0, 7)
      asm volatile("s_waitcnt vmcnt(%0)" ::"n"(6 - k) : "memory");
      LAS char* p = sm + 4096 + w * 7168 + k * 1024 + baddr;
      const v4u f = cat(trd(p), trd(p + 256));
#pragma unroll
      for (int e = 0; e < 4; ++e) { const unsigned u = f[e]; s = dot2(u, one2, s); q = dot2(u, u, q); }
    SEND
    *(LAS float*)(sm + lane * 16 + w * 4) = swap32_add(s, q);
    BARRIER();
    if (t < 32) {
      const f32x4 a = *(LAS f32x4*)(sm + t * 16), c = *(LAS f32x4*)(sm + 512 + t * 16);
      const float m = ((a[0] + a[1]) + (a[2] + a[3])) * (1.f / 448.f);
      const float var = fmaxf(((c[0] + c[1]) + (c[2] + c[3])) * (1.f / 448.f) - m * m, 0.f) + 1e-5f;
      __builtin_amdgcn_raw_buffer_store_b64(v2u{__float_as_uint(m), __float_as_uint(__builtin_amdgcn_rsqf(var))}, rs, t * 8,
                                            WS_ST1 + (ph >> 1) * 16384 + (hb >> 1) * 1024 + (ph & 1) * 512 + (hb & 1) * 256, 16);
    }
  }
  asm volatile("k_fused_code_mark:");
}

#define ACLOB \
  "a0", "a1", "a2", "a3", "a4", "a5", "a6", "a7", "a8", "a9", "a10", "a11", "a12", "a13", "a14", "a15", \
  "a16", "a17", "a18", "a19", "a20", "a21", "a22", "a23", "a24", "a25", "a26", "a27", "a28", "a29", "a30", "a31", \
  "a32", "a33", "a34", "a35", "a36", "a37", "a38", "a39", "a40", "a41", "a42", "a43", "a44", "a45", "a46", "a47", \
  "a48", "a49", "a50", "a51", "a52", "a53", "a54", "a55", "a56", "a57", "a58", "a59", "a60", "a61", "a62", "a63", \
  "a64", "a65", "a66", "a67", "a68", "a69", "a70", "a71", "a72", "a73", "a74", "a75", "a76", "a77", "a78", "a79", \
  "a80", "a81", "a82", "a83", "a84", "a85", "a86", "a87", "a88", "a89", "a90", "a91", "a92", "a93", "a94", "a95", \
  "a96", "a97", "a98", "a99", "a100", "a101", "a102", "a103", "a104", "a105", "a106", "a107", "a108", "a109", "a110", "a111", \
  "a112", "a113", "a114", "a115", "a116", "a117", "a118", "a119", "a120", "a121", "a122", "a123", "a124", "a125", "a126", "a127", \
  "a128", "a129", "a130", "a131", "a132", "a133", "a134", "a135", "a136", "a137", "a138", "a139", "a140", "a141", "a142", "a143", \
  "a144", "a145", "a146", "a147", "a148", "a149", "a150", "a151", "a152", "a153", "a154", "a155", "a156", "a157", "a158", "a159", \
  "a160", "a161", "a162", "a163", "a164", "a165", "a166", "a167", "a168", "a169", "a170", "a171", "a172", "a173", "a174", "a175", \
  "a176", "a177", "a178", "a179", "a180", "a181", "a182", "a183", "a184", "a185", "a186", "a187", "a188", "a189", "a190", "a191", \
  "a192", "a193", "a194", "a195", "a196", "a197", "a198", "a199", "a200", "a201", "a202", "a203", "a204", "a205", "a206", "a207", \
  "a208", "a209", "a210", "a211", "a212", "a213", "a214", "a215", "a216", "a217", "a218", "a219", "a220", "a221", "a222", "a223", \
  "a224", "a225", "a226", "a227", "a228", "a229", "a230", "a231", "a232", "a233", "a234", "a235", "a236", "a237", "a238", "a239", \
  "a240", "a241", "a242", "a243", "a244", "a245", "a246", "a247", "a248", "a249", "a250", "a251", "a252", "a253", "a254", "a255"
struct Ctx {
  int t, lane, v16, v16b, t16, xvoff, sigv, polv, hvoff, gvoff;
  LAS char *abase, *abase8, *bbase, *pw, *prd, *udst, *trb;
  int w, b, pan, ft, lt, dt;
};
template <class Hook> DI void conv_epi_a(f32x4 (&acc)[8], const Ctx& x, int half, Hook&& hook) {
  asm volatile("s_nop 7" ::: "memory");
  SFOR(c, 0, 4) *(LAS f32x4*)(x.pw + c * 1024) = acc[4 * half + c]; hook(c_); SEND
}
DI void conv_epi_b(const float* base, const float (&kscale)[2], const Ctx& x, rsrc_t rs, int soff) {
  unsigned o[2][2];
#pragma unroll
  for (int c = 0; c < 2; ++c) {
    const f32x4 y = *(LAS f32x4*)(x.prd + c * 4096) + *(LAS f32x4*)(x.prd + c * 4096 + 8192);
#pragma unroll
    for (int e = 0; e < 2; ++e) o[c][e] = pkbf(base[4 * c + 2 * e] + y[2 * e] * kscale[c], base[4 * c + 2 * e + 1] + y[2 * e + 1] * kscale[c]);
  }
  const bool odd = x.t & 1;
  v4u ov;
#pragma unroll
  for (int e = 0; e < 2; ++e) { const unsigned n0 = nb1(o[0][e]), n1 = nb1(o[1][e]); ov[e] = odd ? n1 : o[0][e]; ov[2 + e] = odd ? o[1][e] : n0; }
  st16(ov, rs, x.xvoff, soff);
}
constexpr int wd_slot(int k) { return k < NV + 8 ? 56 + k - NV : k - NV - 8; }
constexpr int HS = 28;
template <bool LAST, int I> DI void down_step(f32x16& a0, f32x16& a1, v4u (&wd1)[NV]) {
  if constexpr (2 * I < NV) {
    if (LAST) { mf_va<false, HS + I>(a0, wd1[2 * I]); mf_va<false, HS + I>(a1, wd1[2 * I + 1]); }
    else { mf_av<false, HS + I>(a0, wd1[2 * I]); mf_av<false, HS + I>(a1, wd1[2 * I + 1]); }
  } else {
    if (LAST) { mf_aa<wd_slot(2 * I), HS + I>(a0); mf_aa<wd_slot(2 * I + 1), HS + I>(a1); }
    else { mf_aa<HS + I, wd_slot(2 * I)>(a0); mf_aa<HS + I, wd_slot(2 * I + 1)>(a1); }
  }
}
DI void MF_VV0(f32x16& acc, v4u a, v4u b) { asm volatile(MFOP "%1, %2, 0" : "=&v"(acc) : "v"(a), "v"(b)); }
template <bool LAST> DI void down_init(f32x16& a0, f32x16& a1, v4u id0, v4u id1, const Ctx& x, int half) {
  LAS char* rp = x.trb + (L_RES - L_BUF) + (x.w * 2 + half) * 1024;
  const v4u xf = cat(trd(rp), trd(rp + 256));
  if (LAST) { MF_VV0(a0, id0, xf); MF_VV0(a1, id1, xf); } else { MF_VV0(a0, xf, id0); MF_VV0(a1, xf, id1); }
}
template <bool LAST, class Hook>
DI void down_tail(f32x16& a0, f32x16& a1, LAS char* pw, LAS char* pr, const Ctx& x, LAS char* sm, char* ws, u16* out, rsrc_t rs, int half, Hook&& hook) {
  pw += half * 4 * G2S; pr += half * 4 * G2S;
  SFOR(qd, 0, 4)
    *(LAS f32x4*)(pw + qd * 32) = f32x4{a0[4 * qd], a0[4 * qd + 1], a0[4 * qd + 2], a0[4 * qd + 3]};
    hook(std::integral_constant<int, 2 * qd>{});
    *(LAS f32x4*)(pw + qd * 32 + (LAST ? 128 : 32 * 144)) = f32x4{a1[4 * qd], a1[4 * qd + 1], a1[4 * qd + 2], a1[4 * qd + 3]};
    hook(std::integral_constant<int, 2 * qd + 1>{});
  SEND
  hook(std::integral_constant<int, 8>{});
  f32x4 yp[8];
#pragma unroll
  for (int ww = 0; ww < 4; ++ww) { yp[2 * ww] = *(LAS f32x4*)(pr + ww * G2S); yp[2 * ww + 1] = *(LAS f32x4*)(pr + ww * G2S + 16); }
  hook(std::integral_constant<int, 9>{});
  const f32x4 y0 = (yp[0] + yp[2]) + (yp[4] + yp[6]), y1 = (yp[1] + yp[3]) + (yp[5] + yp[7]);
  v4u ov = {pkbf(y0[0], y0[1]), pkbf(y0[2], y0[3]), pkbf(y1[0], y1[1]), pkbf(y1[2], y1[3])};
  if (LAST) {
    st16(ov, __builtin_amdgcn_make_buffer_rsrc((void*)out, 0, 2048 * 896, 0x00020000), (x.t >> 3) * 896 + (x.t & 7) * 16, (32 * half + x.lt) * 32 * 896 + x.dt * 128);
  } else {
    st16(ov, rs, x.t16, WS_X0 + 2 * WS_XSTR + (32 * half + x.lt) * GSL + x.dt * 4096);
    hook(std::integral_constant<int, 10>{});
    float v[8];
    SFOR(e, 0, 8)
      const float a = (e & 1) ? bfhi(ov[e >> 1]) : bflo(ov[e >> 1]);
      v[e] = swap32_add(a, a * a); PIN(v[e]);
      if constexpr (e % 3 == 2) hook(std::integral_constant<int, 11 + e / 3>{});
    SEND
    SFOR(e, 0, 4)
      v[e] = swap16_add(v[e], v[4 + e]); v[e] = dpp_add<0x124>(v[e]); v[e] = dpp_add<0x128>(v[e]); PIN(v[e]);
      if constexpr (e & 1) hook(std::integral_constant<int, 13 + e / 2>{});
    SEND
    const float m01 = (x.lane & 4) ? v[1] : v[0], m23 = (x.lane & 4) ? v[3] : v[2];
    *(LAS float*)(sm + L_SM + x.v16 + x.w * 4) = (x.lane & 8) ? m23 : m01;
    hook(std::integral_constant<int, 15>{});
    BARRIER();
    const f32x4 s4 = *(LAS f32x4*)(sm + L_SM + x.v16);
    hook(std::integral_constant<int, 16>{});
    asm volatile("s_cmp_lg_u32 %3, 0\n\ts_cbranch_scc1 1f\n\tglobal_atomic_add_f64 %0, %1, %2\n1:"
                 ::"v"(st_idx(8 * (x.lane & 3) + 4 * ((x.lane >> 4) & 1) + ((x.lane >> 2) & 3)) + (x.lane >> 5) * 8),
                 "v"((double)((s4[0] + s4[1]) + (s4[2] + s4[3]))), "s"(ws + WS_ST2 + half * 16384 + x.lt * 128), "s"(x.w) : "scc", "memory");
  }
}
#define ACCWAIT(a, b) asm volatile("s_nop 7\n\ts_nop 3" : "+v"(a), "+v"(b))
template <bool LAST, class Hook, class Fin>
DI void glu_down(v4u (&wd1)[NV], const Ctx& x, LAS char* sm, char* ws, u16* out, rsrc_t rs, unsigned& stampv, Hook&& hook_b, Fin&& hook_t) {
  constexpr int layer = LAST ? 1 : 0;
  const int dvoff = x.v16, hA = WS_H + layer * WS_HSTR + x.lt * 114688 + x.w * 28672, hB = hA + 32 * 114688;
  char* hbo = ws + WS_LOC + (layer * 32 + 16 + (x.lt >> 1)) * 256;
  char* hbs = ws + WS_LOC + (layer * 32 + 16 + x.pan) * 256;
  const int zero = opq(0);
  f32x16 a0, a1, b0, b1;
  unsigned pv;
  v4u id0, id1;
  const int k1 = opq(x.lane & 31) - 16 * x.w - 8 * (x.lane >> 5);
#pragma unroll
  for (int e = 0; e < 4; ++e) {
    id0[e] = k1 == 2 * e ? 0x3f80u : (k1 == 2 * e + 1 ? 0x3f800000u : 0u);
    id1[e] = k1 + 32 == 2 * e ? 0x3f80u : (k1 + 32 == 2 * e + 1 ? 0x3f800000u : 0u);
  }
  LAS char* pw = opq(sm + L_G2 + x.w * G2S + (x.lane & 31) * (LAST ? 272 : 144) + (x.lane >> 5) * 16);
  LAS char* pr = opq(sm + L_G2 + (LAST ? (x.t >> 3) * 272 + (x.t & 7) * 32 : (x.t >> 2) * 144 + (x.t & 3) * 32));
  asm volatile("s_nop 1" : "+v"(id0), "+v"(id1));
  down_init<LAST>(a0, a1, id0, id1, x, 0);
  ld_slot<HS + 12, 12>(rs, dvoff, hA); ld_slot<HS + 13, 13>(rs, dvoff, hA); ld_slot<HS + 14, 14>(rs, dvoff, hA); ld_slot<HS + 15, 15>(rs, dvoff, hA);
  VMW(18); down_step<LAST, 0>(a0, a1, wd1); ld_slot<HS + 16, 16>(rs, dvoff, hA);
  VMW(18); down_step<LAST, 1>(a0, a1, wd1); ld_slot<HS + 17, 17>(rs, dvoff, hA);
  VMW(18); down_step<LAST, 2>(a0, a1, wd1); ld_slot<HS + 18, 18>(rs, dvoff, hA);
  VMW(18); down_step<LAST, 3>(a0, a1, wd1); ld_slot<HS + 19, 19>(rs, dvoff, hA);
  VMW(17); down_step<LAST, 4>(a0, a1, wd1); ld_slot<HS + 20, 20>(rs, dvoff, hA);
  VMW(17); down_step<LAST, 5>(a0, a1, wd1); ld_slot<HS + 21, 21>(rs, dvoff, hA);
  VMW(17); down_step<LAST, 6>(a0, a1, wd1); ld_slot<HS + 22, 22>(rs, dvoff, hA);
  VMW(17); down_step<LAST, 7>(a0, a1, wd1); ld_slot<HS + 23, 23>(rs, dvoff, hA);
  VMW(17); down_step<LAST, 8>(a0, a1, wd1); ld_slot<HS + 24, 24>(rs, dvoff, hA);
  VMW(17); down_step<LAST, 9>(a0, a1, wd1); ld_slot<HS + 25, 25>(rs, dvoff, hA); WAITV(hB, 14); SBAR(); add_one(hbs, zero, x.w, 1ull);
  VMW(16); down_step<LAST, 10>(a0, a1, wd1); ld_slot<HS + 26, 26>(rs, dvoff, hA);
  VMW(16); down_step<LAST, 11>(a0, a1, wd1); ld_slot<HS + 27, 27>(rs, dvoff, hA);
  VMW(15); down_step<LAST, 12>(a0, a1, wd1);
  VMW(14); down_step<LAST, 13>(a0, a1, wd1);
  VMW(13); down_step<LAST, 14>(a0, a1, wd1);
  VMW(12); down_step<LAST, 15>(a0, a1, wd1);
  VMW(11); down_step<LAST, 16>(a0, a1, wd1);
  VMW(10); down_step<LAST, 17>(a0, a1, wd1);
  VMW(9); down_step<LAST, 18>(a0, a1, wd1);
  VMW(8); down_step<LAST, 19>(a0, a1, wd1);
  VMW(7); down_step<LAST, 20>(a0, a1, wd1);
  VMW(6); down_step<LAST, 21>(a0, a1, wd1);
  VMW(5); down_step<LAST, 22>(a0, a1, wd1);
  VMW(4); down_step<LAST, 23>(a0, a1, wd1);
  VMW(3); down_step<LAST, 24>(a0, a1, wd1);
  VMW(2); down_step<LAST, 25>(a0, a1, wd1);
  VMW(1); down_step<LAST, 26>(a0, a1, wd1); poll_issue(pv, hbo, zero, x.w);
  WAITV0(none, 0, x.w); down_step<LAST, 27>(a0, a1, wd1);
  STAMP(58 + 3 * layer);
  down_init<LAST>(b0, b1, id0, id1, x, 1);
  ACCWAIT(a0, a1);
  down_tail<LAST>(a0, a1, pw, pr, x, sm, ws, out, rs, 0, [&](auto n_) AINL {
    constexpr int n = decltype(n_)::value;
    if constexpr (n == 8) { asm volatile("s_waitcnt lgkmcnt(0)" ::: "memory"); poll_check<0>(pv, hbo, zero, 14u, x.w); }
    if constexpr (n == 9) { SFOR(k, 0, 5) ld_slot<HS + k, k>(rs, dvoff, hB); SEND }
    if constexpr (n >= 10) ld_slot<HS + n - 5, n - 5>(rs, dvoff, hB);
  });
  MARK(xA);
  STAMP(60 + 3 * layer);
  constexpr int PRE = PREH;
  SFOR(k, LAST ? 5 : 12, PRE) ld_slot<HS + k, k>(rs, dvoff, hB); SEND
  SFOR(i, 0, 28)
    constexpr int nring = LAST || i < 29 - PRE ? 0 : i - (28 - PRE);
    VMW((i < 28 - PRE ? PRE - 1 : 27 - i) + (i < 5) + nring);
    if constexpr (i == 0) STAMP(59 + 3 * layer);
    down_step<LAST, i>(b0, b1, wd1);
    if constexpr (i + PRE < 28) ld_slot<HS + i + PRE, i + PRE>(rs, dvoff, hB);
    hook_b(std::integral_constant<int, i>{});
  SEND
  STAMP(LAST ? 28 : 15);
  ACCWAIT(b0, b1);
  down_tail<LAST>(b0, b1, pw, pr, x, sm, ws, out, rs, 1, hook_t);
}

__global__ void __launch_bounds__(256, 1)
k_fused(const u16* __restrict__ x0, const u16* __restrict__ lar, const u16* __restrict__ aim, const u16* __restrict__ cre,
        const u16* __restrict__ cim, const u16* __restrict__ ldt, const u16* __restrict__ dsk, const u16* __restrict__ wg,
        const u16* __restrict__ wu, const u16* __restrict__ wd, u16* __restrict__ out, char* __restrict__ ws) {
  __shared__ __attribute__((aligned(16))) char smem[LDS_BYTES];
  if (CODE_PF) {
    unsigned long long pc, keep;
    asm volatile("s_getpc_b64 %0\n9:\n\tv_cmp_gt_i32 vcc, k_fused_code_mark-9b-16, %2\n\ts_and_saveexec_b64 %1, vcc\n\ts_cbranch_execz 8f\n\t"
                 "global_load_dwordx4 a[252:255], %2, %0\n8:\n\ts_mov_b64 exec, %1"
                 : "=&s"(pc), "=&s"(keep) : "v"((blockIdx.x >> 3) * 4096 + threadIdx.x * 16) : "vcc", "scc", "memory");
  }
  LAS char* sm = (LAS char*)smem;
  const unsigned smb = (unsigned)(size_t)sm;
  const rsrc_t rs = __builtin_amdgcn_make_buffer_rsrc((void*)ws, 0, WS_BYTES, 0x00020000);
  asm volatile("" ::: ACLOB);
  if (blockIdx.x >= NB) { helper(x0, wg, wu, ws, rs, sm, smb); return; }
  float kscale[2], dskip[2], base[8];
  unsigned stampv = 0;
  Ctx x;
  RSTAMP0(); STAMP(0);
  {
    const int t = threadIdx.x, lane = t & 63, w = __builtin_amdgcn_readfirstlane(t >> 6), b = blockIdx.x, ch = w >> 1;
    const int q4 = (lane & 15) >> 2, p4 = lane & 3, grp = (lane >> 4) & 1, hh = lane >> 5, i16 = lane & 15, g4 = lane >> 4;
    const int d = 2 * b + ch, n0 = 2 * ((w & 1) * 64 + lane);
    unsigned p_lar, p_aim, p_dt, p_cr, p_ci, o_cr, o_ci, p_dsk;
    asm volatile("global_load_dword %0, %8, %11\n\tglobal_load_dword %1, %8, %12\n\tglobal_load_ushort %2, %10, %13\n\t"
                 "global_load_dword %3, %8, %14\n\tglobal_load_dword %4, %8, %15\n\tglobal_load_dword %5, %9, %14\n\t"
                 "global_load_dword %6, %9, %15\n\tglobal_load_ushort %7, %10, %16"
                 : "=&v"(p_lar), "=&v"(p_aim), "=&v"(p_dt), "=&v"(p_cr), "=&v"(p_ci), "=&v"(o_cr), "=&v"(o_ci), "=&v"(p_dsk)
                 : "v"(d * 512 + n0 * 2), "v"(d * 512 + (n0 ^ 128) * 2), "v"(d * 2), "s"(lar), "s"(aim), "s"(ldt), "s"(cre), "s"(cim), "s"(dsk) : "memory");
    unsigned long long tag, zv;
    tag_issue(tag, ws, WS_EZ);
    asm volatile("buffer_store_dword %0, %1, %2, 0 offen sc1" ::"v"(0), "v"(t < 16 && b < 8 ? WS_CTL + (t * 8 + b) * 256 : WS_BYTES), "s"(rs) : "memory");
    const int lt0 = b / 7, dt0 = b % 7;
#pragma unroll
    for (int k = 0; k < 2; ++k)
      dma16g((const char*)x0 + (64 * lt0 + 16 * w + 8 * k) * 896 + dt0 * 128, (lane >> 3) * 896 + ((lane ^ (lane >> 4)) & 7) * 16, smb + L_XT + (2 * w + k) * 1024);
    const int cid0 = 4 * b + w, cid = cid0 < 784 ? cid0 : cid0 - 784, ks = cid / 28, cp = cid % 28, S = cid / 7, dtw = cid % 7;
    const char* wsrc[3] = {(const char*)wg + 16 * ks * 3584 + cp * 128, (const char*)wu + 16 * ks * 3584 + cp * 128, (const char*)wd + 16 * S * 896 + dtw * 128};
    const int wv01 = (lane >> 3) * 3584 + (lane & 7) * 16, wv2 = (lane >> 3) * 896 + (lane & 7) * 16;
    auto wdma = [&](int n) AINL { const int k = n >> 1, h = n & 1; dma16g(wsrc[k] + 8 * h * (k < 2 ? 3584 : 896), k < 2 ? wv01 : wv2, smb + L_WR + (w * 3 + k) * 2048 + h * 1024); };
    SFOR(n, 0, 4) wdma(n); SEND
    STAMP(32);
    int goff[11], soff[4], soff2[4];
#pragma unroll
    for (int k = 0; k < 11; ++k) {
      const int lag = 2048 - 8 * t - k;
      goff[k] = L_TP + 8 + 2 * (lag < 128 ? lag : (int)(pkbf((float)lag, 0.f) & 0xffffu) - 0x4280);
      PIN(goff[k]);
    }
    {
      const int tl = w & 1, s01 = opq(128 * tl + 4 * g4 + 8 * i16), s2 = opq(256 + 64 * tl + 4 * i16), s3 = opq(384 + 32 * tl + 2 * i16);
      const int sb = L_TP + ch * TP_CH + 8 + 2 * (g4 < 2 ? s01 : (g4 == 2 ? s2 : s3)), sx = g4 == 3 ? 124 : 0;
#pragma unroll
      for (int r = 0; r < 4; ++r) {
        soff[r] = sb + 2 * r + (r >> 1) * sx; PIN(soff[r]);
        const int p2 = 512 + 16 * (4 * g4 + r) + i16;
        soff2[r] = L_TP + ch * TP_CH + 8 + 2 * (p2 < 648 ? p2 : 648); PIN(soff2[r]);
      }
      const int xcd = b & 7, jj = b >> 3;
      x.t = t; x.lane = lane; x.w = w; x.b = b;
      x.lt = 4 * xcd + jj / 7; x.dt = jj % 7;
      x.pan = __builtin_amdgcn_readfirstlane(MAP47 ? 4 * (xcd >> 1) + jj / 7 : 2 * xcd + jj / 14);
      x.ft = __builtin_amdgcn_readfirstlane(MAP47 ? 7 * (xcd & 1) + x.dt : jj % 14);
      x.v16 = opq(lane * 16); x.t16 = opq(t * 16); x.xvoff = opq(x_voff(t, b));
      x.sigv = opq(sig_off(lane, b)); x.polv = opq(pol_off(lane, b));
      x.hvoff = opq(lane * 16 + (4 * x.ft + w) * 2048); x.gvoff = opq(x.pan * 1024 + (lane & 31) * 8); x.v16b = opq(lane * 16 + 4096);
      x.abase = opq(sm + L_KC + ch * KC_CH + ((-i16) & 3) * KC_COPY + 2 * (2032 + 8 * g4 - 4 * ((i16 + 3) >> 2)) - 3584 - 192 - 256 * (w & 1));
      x.abase8 = opq(x.abase + 8);
      x.bbase = opq(sm + L_U + ch * U_CH + 2 * (240 + 16 * i16 + 8 * g4) - 192 - 256 * (w & 1));
      x.pw = opq(sm + L_PS + ((w & 1) * 2 + ch) * 4096 + 64 * i16 + 16 * (g4 ^ ((i16 >> 1) & 3)));
      x.prd = opq(sm + L_PS + w * 1024 + 64 * (lane >> 2) + 16 * (p4 ^ ((lane >> 3) & 3)));
      x.udst = opq(sm + L_U + 512 + t * 8);
      x.trb = opq(sm + L_BUF + (8 * hh + q4) * 64 + (4 * grp + p4) * 8);
    }
    LAS char* vw = opq(sm + L_VW + ch * VW_CH + 8 * ((w & 1) * 64 + lane));
    LAS char* pa = opq(sm + L_VW + ch * VW_CH + i16 * VW_ROW + g4 * 256);
    LAS char* p0 = opq(pa + (w & 1) * 16 * VW_ROW);
    LAS char* wr0 = opq(sm + L_WR + w * 6144 + (8 * hh + q4) * 128 + 32 * grp + 8 * p4);
    LAS char* wr2 = opq(sm + L_WR + w * 6144 + 4096 + (4 * hh + q4) * 128 + 32 * grp + 8 * p4);
    const int wdst01 = opqs(WS_WGU + 2 * cp * 57344 + 2 * ks * 1024), wdst2 = opqs(WS_WD + ((dtw * 4 + S / 28) * 56 + 2 * (S % 28)) * 1024);
    const int zoff = opq(WS_EZ + (1 + (lane & 7)) * 256);
    LAS char* xt0 = opq(sm + L_XT + (8 * g4 + q4) * 128 + ((((4 * w + p4) >> 1) ^ (q4 >> 1)) * 16) + (p4 & 1) * 8);
    LAS char* xt1 = opq(sm + L_XT + (8 * g4 + q4 + 4) * 128 + ((((4 * w + p4) >> 1) ^ (2 + (q4 >> 1))) * 16) + (p4 & 1) * 8);
    const int xto = opq((64 * dt0 + 16 * w + i16) * 64 + g4 * 16);
    STAMP(33);
    asm volatile("s_waitcnt vmcnt(6)" : "+v"(p_lar), "+v"(p_aim), "+v"(p_dt), "+v"(p_cr), "+v"(p_ci), "+v"(o_cr), "+v"(o_ci), "+v"(p_dsk), "+v"(tag)::"memory");
    tag += 1;
    asm volatile("buffer_store_dwordx2 %0, %1, %2, 0 offen sc1" ::"v"(tag), "v"(t == 0 && b < 8 ? WS_EZ + (1 + b) * 256 : WS_BYTES), "s"(rs) : "memory");
    const float dt = rbf(expf(bflo(p_dt)));
    const f32x2 lr = {-rbf(expf(bflo(p_lar))) * dt * 1.44269504f, -rbf(expf(bfhi(p_lar))) * dt * 1.44269504f};
    const f32x2 rev = {bflo(p_aim) * dt * 0.159154943f, bfhi(p_aim) * dt * 0.159154943f};
    const Cx z1 = cpow(lr, rev, 1.f), z8 = cpow(lr, rev, 8.f), z128 = cpow(lr, rev, 128.f);
    unsigned E;
    {
      const unsigned a = max(p_cr & 0x7fffu, p_ci & 0x7fffu), c = max(p_cr & 0x7fff0000u, p_ci & 0x7fff0000u);
      const unsigned a2 = max(o_cr & 0x7fffu, o_ci & 0x7fffu), c2 = max(o_cr & 0x7fff0000u, o_ci & 0x7fff0000u);
      E = __builtin_amdgcn_readlane(wave_max63(max(max(a, a2), max(c, c2) >> 16)), 63) >> 7;
      E = E > 253u ? 253u : E;
    }
    const float sc = __uint_as_float((254u - E) << 23);
    if (lane == 0) *(LAS f32x2*)(sm + L_TP + 2 * TP_CH + 8 * ch) = f32x2{2.f * dt * __uint_as_float(E << 23), bflo(p_dsk)};
    STAMP(34);
    Cx v[8], c = {{1.f, 1.f}, {0.f, 0.f}};
    const Cx z8c = {z8.r, -z8.i};
    v[0] = Cx{{bflo(p_cr) * sc, bfhi(p_cr) * sc}, {bflo(p_ci) * sc, bfhi(p_ci) * sc}};
    SFOR(r, 0, 8)
      put_vw<41 + r>(vw, c); c = r ? cmul(c, z8c) : z8c;
      put_vw<r>(vw, v[r]); if constexpr (r < 7) v[r + 1] = cmul(v[r], z1);
    SEND
    auto rows16 = [&](auto r_) AINL {
      constexpr int r = decltype(r_)::value;
      v[r] = cmul(v[r], z128); put_vw<16 + r>(vw, v[r]);
      put_vw<49 + r>(vw, c); if constexpr (r < 7) c = cmul(c, z8c);
    };
    SFOR(r, 0, V1 ? 4 : 8) rows16(r_); SEND
    STAMP(35);
    asm volatile("s_waitcnt vmcnt(1)\n\ts_barrier" ::: "memory");
    STAMP(36);
    tag_issue(zv, ws, zoff);
#pragma unroll
    for (int sl = 0; sl < 2; ++sl) st16(cat(trd(xt0 + sl * 4096), trd(xt1 + sl * 4096)), rs, xto, WS_X0 + (2 * lt0 + sl) * GSL);
    MARK(x0s);
    const int wvo = cid0 < 784 ? x.v16 : WS_BYTES;
    auto wstore = [&](int k, int hf) AINL {
      LAS char* p = (k < 2 ? wr0 + k * 2048 : wr2) + 64 * hf;
      st16(cat(trd(p), trd(p + (k < 2 ? 4 : 8) * 128)), rs, wvo, k < 2 ? wdst01 + hf * 57344 + k * 1024 : wdst2 + hf * 1024);
    };
    wstore(0, 0); wstore(0, 1); wstore(1, 0); wstore(1, 1);
    wdma(4); wdma(5);
    STAMP(1);
    SFOR(r, V1 ? 4 : 8, 8) rows16(r_); SEND
    SFOR(r, 0, 4) v[2 * r] = cmul(v[2 * r], z128); put_vw<8 + r>(vw, v[2 * r]); SEND
    if (!V1) { SFOR(r, 0, 4) v[2 * r] = cmul(v[2 * r], z128); put_vw<24 + r>(vw, v[2 * r]); SEND }
    VMW(V2 ? 2 : 6);
    PIN(zv);
    zero_check(zv, tag, ws, zoff, w);
    SBAR();
    add_one(ctr(ws, C_A), x.sigv, w, 0xffull);
    if (V2) add_one(ctr(ws, C_W), x.sigv, w, 0xffull);
    STAMP(37);
    if (V1) { SFOR(r, 0, 4) v[2 * r] = cmul(v[2 * r], z128); put_vw<24 + r>(vw, v[2 * r]); SEND }
    {
      const Cx z256 = cmul(z128, z128), z512 = cmul(z256, z256);
      Cx u = cmul(cmul(v[0], z128), z512);
      SFOR(k, 0, 8)
        constexpr int r = k & 1, st = k >> 1;
        put_vw<32 + k>(vw, u); u = cmul(u, z128);
        v[4 * r] = cmul(v[4 * r], z128); put_vw<12 + 16 * (st & 1) + 2 * (st >> 1) + r>(vw, v[4 * r]);
      SEND
      put_vw<40>(vw, u);
    }
    STAMP(39);
    VMW(0);
    BARRIER();
    if (!V2) add_one(ctr(ws, C_W), x.sigv, w, 0xffull);
    wstore(2, 0); wstore(2, 1);
    STAMP(2);
    unsigned pv, pv2;
    Pro pr, pr2;
    {
      f32x4 k0, k2;
      v4u fw[4], f0[4], f2[4];
      auto rd = [&](int k) AINL { fw[k % 4] = *(LAS v4u*)(pa + 41 * VW_ROW + k * 16); f0[k % 4] = *(LAS v4u*)(p0 + k * 16); f2[k % 4] = *(LAS v4u*)(pa + 32 * VW_ROW + k * 16); };
      rd(0); rd(1); rd(2);
      SFOR(kk, 0, 16)
        if (kk == 0) { MF16_0(k0, f0[0], fw[0]); MF16_0(k2, f2[0], fw[0]); } else { MF16(k0, f0[kk % 4], fw[kk % 4]); MF16(k2, f2[kk % 4], fw[kk % 4]); }
        if (kk + 3 < 16) rd(kk + 3);
      SEND
      asm volatile("s_nop 7" : "+v"(k0), "+v"(k2));
#pragma unroll
      for (int r = 0; r < 4; ++r) { *(LAS u16*)(sm + soff[r]) = (u16)pkh(k0[r], k0[r]); *(LAS u16*)(sm + soff2[r]) = (u16)pkh(k2[r], k2[r]); }
      if (lane == 0) *(LAS v2u*)(sm + L_TP + ch * TP_CH) = v2u{0u, 0u};
    }
    STAMP(41);
    poll_issue(pv, ctr(ws, C_A), x.polv, w);
    BARRIER();
    STAMP(38);
    unsigned e[2][11];
#pragma unroll
    for (int k = 0; k < 22; ++k) e[k / 11][k % 11] = *(LAS u16*)(sm + (k / 11) * TP_CH + goff[k % 11]);
    const f32x4 kd = *(LAS f32x4*)(sm + L_TP + 2 * TP_CH);
    const unsigned k00 = *(LAS u16*)(sm + L_TP + ((t >> 3) & 1) * TP_CH + 8);
    const int rsoff = WS_WGU + (4 * x.ft + w) * 57344;
    SFOR(c, 0, 2)
      kscale[c] = __uint_as_float(__builtin_amdgcn_readfirstlane(__float_as_uint(kd[2 * c])));
      dskip[c] = __uint_as_float(__builtin_amdgcn_readfirstlane(__float_as_uint(kd[2 * c + 1])));
      unsigned A[5], B[5];
#pragma unroll
      for (int k = 0; k < 5; ++k) { A[k] = e[c][2 * k] | (e[c][2 * k + 1] << 16); B[k] = e[c][2 * k + 1] | (e[c][2 * k + 2] << 16); }
      LAS char* kc = sm + L_KC + c * KC_CH + x.t16;
      SFOR(k, 0, 4)
        *(LAS v4u*)(kc + k * KC_COPY) = (k & 1) ? v4u{B[k >> 1], B[(k >> 1) + 1], B[(k >> 1) + 2], B[(k >> 1) + 3]} : v4u{A[k >> 1], A[(k >> 1) + 1], A[(k >> 1) + 2], A[(k >> 1) + 3]};
        if constexpr (c == 1) { if constexpr (k < 2) pro0_load<1 + k>(pr, rs, x.xvoff, x.t16, 0); else pro0_load<k - 2>(pr2, rs, x.xvoff, x.t16, 1); }
      SEND
      if constexpr (c == 0) {
        poll_check<0>(pv, ctr(ws, C_A), x.polv, (NB + NH) / 8, w);
        STAMP(42);
        poll_issue(pv2, ctr(ws, C_W), x.polv, w);
        pro0_load<0>(pr, rs, x.xvoff, x.t16, 0);
      }
    SEND
    if (t < 16) *(LAS v4u*)(sm + L_KC + (t >> 3) * KC_CH + ((t >> 1) & 3) * KC_COPY + 4096 + (t & 1) * 16) = v4u{(t & 7) == 0 ? k00 : 0u, 0u, 0u, 0u};
    if (t >= 64 && t < 128) *(LAS v4u*)(sm + L_U + ((t >> 5) & 1) * U_CH + (t & 31) * 16) = v4u{0u, 0u, 0u, 0u};
    STAMP(3);
    pro0_load<2>(pr2, rs, x.xvoff, x.t16, 1);
    poll_check<6>(pv2, ctr(ws, C_W), x.polv, NB / 8, w);
#define RING(n) ld_slot<n, n>(rs, x.v16, rsoff)
    SFOR(i, 0, 4) RING(i); SEND
    VMW(7);
    pro_finish<true>(pr, base, dskip, x.udst, t & 1, [&](auto j_) AINL { RING(4 + decltype(j_)::value); });
    STAMP(4);
    f32x4 acc[8];
    ConvPart<0, 4, 0, 4, true> cA; cA.abase = x.abase; cA.abase8 = x.abase8; cA.bbase = x.bbase;
    ConvPart<4, 8, 0, 8, true> cB; cB.abase = x.abase; cB.abase8 = x.abase8; cB.bbase = x.bbase;
    BARRIER();
    STAMP(5);
    RING(8); cA.preB(); RING(9); cA.preA();
    cA.run(acc, [&](auto n_) AINL { constexpr int n = decltype(n_)::value; if constexpr (n % 4 == 2) RING(10 + n / 4); });
    STAMP(44);
    conv_epi_a(acc, x, 0, [&](auto c_) AINL { RING(20 + decltype(c_)::value); });
    BARRIER();
    conv_epi_b(base, kscale, x, rs, WS_X0 + WS_XSTR);
    MARK(cA0);
    VMW(25);
    pro_finish<true>(pr2, base, dskip, x.udst + 2048, t & 1, [&](auto j_) AINL { RING(24 + decltype(j_)::value); });
    cB.preA(); RING(28);
    BARRIER();
    cB.preB(); RING(29);
    STAMP(45);
    cB.run(acc, [&](auto n_) AINL {
      constexpr int n = decltype(n_)::value;
      if constexpr (n % 4 == 1) RING(30 + n / 4);
      if constexpr (n == PCA0) poll_issue(pv, ctr(ws, C_CA0), x.polv, w);
      if constexpr (n == 26) { WAITV(cA0, 13); SBAR(); add_one(ctr(ws, C_CA0), x.sigv, w, 0xffull); }
    });
    STAMP(48);
    conv_epi_a(acc, x, 1, [&](auto) AINL {});
    asm volatile("s_waitcnt lgkmcnt(0)" ::: "memory");
    poll_check<1>(pv, ctr(ws, C_CA0), x.polv, (NB + NH) / 8, w);
    STAMP(7);
  }

  v4u wd1[NV];
  const int rsoff0 = WS_WGU + (4 * x.ft + x.w) * 57344, dsoff0 = WS_WD + (x.dt * 4 + x.w) * 57344;
  char* const ws0 = ws;
#pragma unroll 1
  for (int layer = 0;; ++layer) {
    const int rsoff = opqs(rsoff0), dsoff = opqs(dsoff0);
    char* const ws = opqs(ws0);
    x.w = opqs(x.w); x.lt = opqs(x.lt); x.dt = opqs(x.dt); x.pan = opqs(x.pan); x.ft = opqs(x.ft); x.t = opq(x.t); x.lane = opq(x.lane);
    const int xmid = WS_X0 + (2 * layer + 1) * WS_XSTR, hbuf = WS_H + layer * WS_HSTR;
    char* const c_cb = ctr(ws, C_CB0 + 2 * layer);
    unsigned pv;
    {
      const int dvoff = x.v16;
      auto slab_of = [&](int tt) AINL { return (tt >> 1) * 32 + 2 * x.pan + (tt & 1); };
      int so[4];
#pragma unroll
      for (int tt = 0; tt < 4; ++tt) so[tt] = opqs(xmid + slab_of(tt) * GSL + x.w * 7168);
      const unsigned lb = opqs(smb + L_BUF + x.w * 7168);
      auto dma_piece = [&](auto tt_, auto k_) AINL {
        constexpr int tt = decltype(tt_)::value, k = decltype(k_)::value;
        if constexpr ((k & 3) == 0) dma_first<tt * SLAB + k * 1024>(rs, lb, k ? x.v16b : x.v16, so[tt]); else dma_next<k & 3>(rs, k > 4 ? x.v16b : x.v16, so[tt]);
      };
#define DMAP(tt, k) dma_piece(std::integral_constant<int, tt>{}, std::integral_constant<int, k>{})
      DMAP(0, 0); DMAP(0, 1);
      conv_epi_b(base, kscale, x, rs, xmid + 32 * GSL);
      MARK(cB);
      SFOR(k, 2, 7) DMAP(0, k); SEND
      MARK(s0);
      unsigned csg, csu;
      asm volatile("buffer_load_dword %0, %2, %3, %4 offen sc1\n\tbuffer_load_dword %1, %2, %3, %5 offen sc1"
                   : "=&v"(csg), "=&v"(csu) : "v"((x.lane & 31) * 4), "s"(rs), "s"(WS_CS + (4 * x.ft + x.w) * 128), "s"(WS_CS + 7168 + (4 * x.ft + x.w) * 128) : "memory");
      MARK(cs);
      int dcur = opqs(dsoff);
      ld_reg_c<0>(wd1[0], rs, dvoff, dcur); ld_reg_c<1>(wd1[1], rs, dvoff, dcur);
      WAITV(cB, 9);
      SBAR();
      add_one(c_cb, x.sigv, x.w, 0xffull);
      SFOR(k, 0, 7) DMAP(1, k); SEND
      ld_reg_c<2>(wd1[2], rs, dvoff, dcur); ld_reg_c<3>(wd1[3], rs, dvoff, dcur); adv(dcur);
      char* const gbase = opqs(ws + WS_ST1 + layer * 16384);
      char* const hao = opqs(ws + WS_LOC + (layer * 32 + (x.lt >> 1)) * 256);
      char* const has = opqs(ws + WS_LOC + (layer * 32 + x.pan) * 256);
      const int hA = opqs(hbuf + x.lt * 114688 + x.w * 28672), zero = opq(0);
      int hso[4], rso[2];
#pragma unroll
      for (int tt = 0; tt < 4; ++tt) hso[tt] = opqs(hbuf + slab_of(tt) * 114688);
#pragma unroll
      for (int h = 0; h < 2; ++h) rso[h] = opqs(xmid + (32 * h + x.lt) * GSL + (64 * x.dt + 16 * x.w) * 64);
      const unsigned lr = opqs(smb + L_RES + x.w * 2048);
      LAS char* trb2 = opq(x.trb + 2 * SLAB);
      f32x16 ag[2], au[2];
      v4u bf[3], baug = {0u, 0u, 0u, 0u}, hp, aug_g, aug_u;
      v2u gr;
      float nrl[2], sd[2], tm[16];
      f32x2 tp[8];
      PIN(baug);
      WAITV0(cs, 11, x.w);
      {
        PIN(csg); PIN(csu);
        const float g = __uint_as_float(csg), gh = rbf(g), u = __uint_as_float(csu), uh = rbf(u);
        aug_g = v4u{x.lane >= 32 ? 0u : pkbf(gh, g - gh), x.lane >= 32 ? 0u : pkbf(gh, 0.f), 0u, 0u};
        aug_u = v4u{x.lane >= 32 ? 0u : pkbf(uh, u - uh), x.lane >= 32 ? 0u : pkbf(uh, 0.f), 0u, 0u};
      }
      auto rdb = [&](int s) AINL {
        LAS char* p = (s < 56 ? x.trb : trb2) + ((s / 28) & 1) * SLAB + (s % 28) * 1024;
        bf[s % 3] = cat(trd(p), trd(p + 256));
      };
#define TR(s) rdb(s)
#define MG(s) mf_av<(s) % 28 == 0, 2 * ((s) % 28)>(ag[((s) / 28) & 1], bf[(s) % 3])
#define MU(s) mf_av<(s) % 28 == 0, 2 * ((s) % 28) + 1>(au[((s) / 28) & 1], bf[(s) % 3])
#define AUG(tp) do { MF_VV(ag[(tp) & 1], aug_g, baug); MF_VV(au[(tp) & 1], aug_u, baug); } while (0)
#define E1(tp, e) v_mul(tm[e], ag[(tp) & 1][e], nrl[(tp) & 1])
#define E5(tp, e) v_mul(tm[e], au[(tp) & 1][e], tm[e])
#define CV(k) do { unsigned p_; v_pk(p_, tm[2 * (k)], tm[2 * (k) + 1]); hp[(k) & 3] = p_; } while (0)
#define HST(tp, s) st16<(s) * 1024>(hp, rs, x.hvoff, hso[tp])
#define RESD(h) dma_first<(h) * 1024>(rs, lr, dvoff, rso[h])
#define GISS(tp) gran_issue<256 * (tp)>(gr, gbase, x.gvoff)
#define GCHK(n, tp) gran_check<n, 256 * (tp)>(gr, gbase, x.gvoff)
#define PREP1() do { unsigned p0_, p1_; float t_; \
        asm volatile("v_cvt_pk_bf16_f32 %0, -%3, -%3\n\tv_lshlrev_b32 %2, 16, %0\n\tv_add_f32 %2, %2, %3\n\tv_cvt_pk_bf16_f32 %1, -%2, 0" \
                     : "=&v"(p0_), "=&v"(p1_), "=&v"(t_) : "v"(gr.x)); baug[0] = p0_; baug[1] = p1_; PIN(baug); } while (0)
#define PREP2(tp) asm volatile("v_mul_f32 %0, 0xbfb8aa3b, %2\n\tv_rcp_f32 %1, %2" : "=&v"(nrl[(tp) & 1]), "=&v"(sd[(tp) & 1]) : "v"(gr.y))
#define PAIR(v, e) f32x2{(v)[2 * (e)], (v)[2 * (e) + 1]}
#define P1(e) asm volatile("v_pk_mul_f32 %0, %1, %2 op_sel_hi:[1,0]" : "=v"(tp[e]) : "v"(PAIR(ag[1], e)), "v"(f32x2{nrl[1], sd[1]}))
#define P2(e) do { float a_ = tp[e][0], b_ = tp[e][1]; v_exp(a_); v_exp(b_); tp[e] = f32x2{a_, b_}; } while (0)
#define P3(e) asm volatile("v_pk_fma_f32 %0, %0, %1, %1 op_sel:[0,1,1]" : "+v"(tp[e]) : "v"(f32x2{nrl[1], sd[1]}))
#define P4(e) do { float a_ = tp[e][0], b_ = tp[e][1]; v_rcp(a_); v_rcp(b_); tp[e] = f32x2{a_, b_}; } while (0)
#define P5(e) asm volatile("v_pk_mul_f32 %0, %1, %0" : "+v"(tp[e]) : "v"(PAIR(au[1], e)))
#define P6(e) do { unsigned p_; v_pk(p_, tp[e][0], tp[e][1]); hp[(e) & 3] = p_; } while (0)
#define PINW() SFOR(k, 0, NV) PIN(wd1[k]); SEND
      SBAR();
      STAMP(9 + 13 * layer);
      TR(0); TR(1);
      PIN(aug_g); PIN(aug_u);
               MG(0); TR(2); MU(0);
      MG(1); TR(3); ld_reg_c<4>(wd1[4], rs, dvoff, dcur); MU(1);
      MG(2); TR(4); ld_reg_c<5>(wd1[5], rs, dvoff, dcur); MU(2);
      MG(3); TR(5); MU(3);
      MG(4); TR(6); ld_reg_c<6>(wd1[6], rs, dvoff, dcur); MU(4);
      MG(5); TR(7); ld_reg_c<7>(wd1[7], rs, dvoff, dcur); MU(5); adv(dcur);
      MG(6); TR(8); ld_reg_c<8>(wd1[8], rs, dvoff, dcur); MU(6);
      MG(7); TR(9); MU(7);
      MG(8); TR(10); ld_reg_c<9>(wd1[9], rs, dvoff, dcur); MU(8);
      MG(9); TR(11); ld_reg_c<10>(wd1[10], rs, dvoff, dcur); MU(9);
      MG(10); TR(12); ld_reg_c<11>(wd1[11], rs, dvoff, dcur); MU(10); adv(dcur);
      MG(11); TR(13); MU(11);
      MG(12); TR(14); ld_reg_c<12>(wd1[12], rs, dvoff, dcur); MU(12);
      MG(13); TR(15); ld_reg_c<13>(wd1[13], rs, dvoff, dcur); MU(13);
      MG(14); TR(16); ld_reg_c<14>(wd1[14], rs, dvoff, dcur); MU(14);
      MG(15); TR(17); MU(15);
      MG(16); TR(18); ld_reg_c<15>(wd1[15], rs, dvoff, dcur); MU(16); adv(dcur);
      MG(17); TR(19); ld_reg_c<16>(wd1[16], rs, dvoff, dcur); MU(17);
      MG(18); TR(20); ld_reg_c<17>(wd1[17], rs, dvoff, dcur); MU(18);
      MG(19); TR(21); MU(19);
      MG(20); TR(22); ld_reg_c<18>(wd1[18], rs, dvoff, dcur); MU(20);
      VMW(17); SBAR();
      MG(21); TR(23); ld_reg_c<19>(wd1[19], rs, dvoff, dcur); MU(21); poll_issue(pv, c_cb, x.polv, x.w); adv(dcur);
      MG(22); TR(24); GISS(0); MU(22);
      MG(23); TR(25); ld_reg_c<20>(wd1[20], rs, dvoff, dcur); MU(23);
      MG(24); TR(26); ld_reg_c<21>(wd1[21], rs, dvoff, dcur); MU(24);
      MG(25); TR(27); ld_reg_c<22>(wd1[22], rs, dvoff, dcur); MU(25);
      MG(26); TR(28); MU(26);
      MG(27); TR(29); ld_reg_c<23>(wd1[23], rs, dvoff, dcur); MU(27); adv(dcur); STAMP(10 + 13 * layer);
      MG(28); TR(30); ld_reg_c<24>(wd1[24], rs, dvoff, dcur); MU(28); GCHK(5, 0);
      MG(29); TR(31); PREP1(); MU(29); PREP2(0);
      AUG(0);
      MG(30); TR(32); ld_reg_c<25>(wd1[25], rs, dvoff, dcur); MU(30);
      MG(31); TR(33); MU(31); poll_check<7>(pv, c_cb, x.polv, NB / 8, x.w);
      MG(32); TR(34); DMAP(2,0); MU(32); E1(0,0); E1(0,1); v_exp(tm[0]); E1(0,2);
      MG(33); TR(35); DMAP(2,1); MU(33); v_exp(tm[1]); v_fma1(tm[0], sd[0]); E1(0,3); v_fma1(tm[1], sd[0]);
      MG(34); TR(36); DMAP(2,2); MU(34); v_exp(tm[2]); v_rcp(tm[0]); E1(0,4);
      MG(35); TR(37); DMAP(2,3); MU(35); v_exp(tm[3]); v_fma1(tm[2], sd[0]); v_rcp(tm[1]);
      MG(36); TR(38); DMAP(2,4); MU(36); E5(0,0); E1(0,5); v_exp(tm[4]); v_fma1(tm[3], sd[0]);
      MG(37); TR(39); DMAP(2,5); MU(37); v_rcp(tm[2]); E5(0,1); E1(0,6); v_fma1(tm[4], sd[0]);
      MG(38); TR(40); DMAP(2,6); MU(38); v_exp(tm[5]); v_rcp(tm[3]); E5(0,2);
      MG(39); TR(41); DMAP(3,0); MU(39); CV(0); E1(0,7); v_exp(tm[6]); v_fma1(tm[5], sd[0]);
      MG(40); TR(42); DMAP(3,1); MU(40); v_rcp(tm[4]); E5(0,3); E1(0,8); v_fma1(tm[6], sd[0]);
      MG(41); TR(43); DMAP(3,2); MU(41); v_exp(tm[7]); v_rcp(tm[5]); E5(0,4);
      MG(42); TR(44); DMAP(3,3); MU(42); CV(1); E1(0,9); v_exp(tm[8]); v_fma1(tm[7], sd[0]);
      MG(43); TR(45); DMAP(3,4); MU(43); v_rcp(tm[6]); E5(0,5); E1(0,10); v_fma1(tm[8], sd[0]);
      MG(44); TR(46); DMAP(3,5); MU(44); v_exp(tm[9]); v_rcp(tm[7]); E5(0,6); CV(2);
      MG(45); TR(47); DMAP(3,6); MU(45); E1(0,11); v_exp(tm[10]); v_fma1(tm[9], sd[0]); v_rcp(tm[8]);
      MG(46); TR(48); E5(0,7); E1(0,12); v_exp(tm[11]); MU(46); v_fma1(tm[10], sd[0]); v_rcp(tm[9]); E5(0,8); CV(3); E1(0,13);
      MG(47); TR(49); GISS(1); v_fma1(tm[11], sd[0]); MU(47); HST(0,0); E5(0,9);
      MG(48); TR(50); ld_reg_c<26>(wd1[26], rs, dvoff, dcur); MU(48); v_exp(tm[12]); v_rcp(tm[10]); E1(0,14); v_fma1(tm[12], sd[0]);
      MG(49); TR(51); v_exp(tm[13]); v_rcp(tm[11]); MU(49); E5(0,10); CV(4); E1(0,15); v_exp(tm[14]); v_fma1(tm[13], sd[0]);
      MG(50); TR(52); v_rcp(tm[12]); E5(0,11); v_fma1(tm[14], sd[0]); MU(50); v_exp(tm[15]); v_rcp(tm[13]); E5(0,12); CV(5);
      MG(51); TR(53); ld_reg_c<27>(wd1[27], rs, dvoff, dcur); MU(51); adv(dcur); v_fma1(tm[15], sd[0]); v_rcp(tm[14]); E5(0,13); E5(0,14);
      MG(52); TR(54); v_rcp(tm[15]); CV(6); E5(0,15); MU(52); CV(7);
      MG(53); TR(55); MU(53);
      VMW(11); SBAR();
      MG(54); TR(56); RESD(0); MU(54); GCHK(4, 1);
      MG(55); TR(57); PREP1(); MU(55); PREP2(1); STAMP(11 + 13 * layer); HST(0,1);
      AUG(1);
      MG(56); TR(58); ld_reg_c<28>(wd1[28], rs, dvoff, dcur); MU(56);
      MG(57); TR(59); ld_reg_c<29>(wd1[29], rs, dvoff, dcur); MU(57);
      MG(58); TR(60); E1(1,0); E1(1,1); v_exp(tm[0]); MU(58); E1(1,2); v_exp(tm[1]); v_fma1(tm[0], sd[1]); E1(1,3);
      MG(59); TR(61); v_exp(tm[2]); v_fma1(tm[1], sd[1]); E1(1,4); MU(59); v_rcp(tm[0]); v_exp(tm[3]); v_fma1(tm[2], sd[1]);
      MG(60); TR(62); v_rcp(tm[1]); E5(1,0); E1(1,5); MU(60); v_exp(tm[4]); v_fma1(tm[3], sd[1]); v_rcp(tm[2]);
      MG(61); TR(63); E5(1,1); E1(1,6); v_exp(tm[5]); MU(61); v_fma1(tm[4], sd[1]); v_rcp(tm[3]); E5(1,2); CV(0);
      MG(62); TR(64); E1(1,7); v_exp(tm[6]); v_fma1(tm[5], sd[1]); MU(62); v_rcp(tm[4]); E5(1,3); E1(1,8); v_fma1(tm[6], sd[1]);
      MG(63); TR(65); v_exp(tm[7]); v_rcp(tm[5]); MU(63); E5(1,4); CV(1); E1(1,9); v_exp(tm[8]);
      MG(64); TR(66); v_fma1(tm[7], sd[1]); v_rcp(tm[6]); E5(1,5); MU(64); E1(1,10); v_exp(tm[9]); v_fma1(tm[8], sd[1]); E5(1,6);
      MG(65); TR(67); v_rcp(tm[7]); CV(2); E1(1,11); MU(65); v_exp(tm[10]); v_fma1(tm[9], sd[1]); v_rcp(tm[8]);
      MG(66); TR(68); E5(1,7); E1(1,12); v_exp(tm[11]); MU(66); v_fma1(tm[10], sd[1]); v_rcp(tm[9]); E5(1,8); CV(3);
      MG(67); TR(69); E1(1,13); v_exp(tm[12]); v_fma1(tm[11], sd[1]); MU(67); HST(1,0);
      MG(68); TR(70); v_rcp(tm[10]); E5(1,9); E1(1,14); MU(68); v_exp(tm[13]); v_fma1(tm[12], sd[1]); v_rcp(tm[11]);
      MG(69); TR(71); E5(1,10); CV(4); E1(1,15); v_fma1(tm[13], sd[1]); MU(69); v_exp(tm[14]); v_rcp(tm[12]); E5(1,11);
      MG(70); TR(72); v_exp(tm[15]); v_fma1(tm[14], sd[1]); E5(1,12); MU(70); v_rcp(tm[13]); CV(5); v_fma1(tm[15], sd[1]); E5(1,13);
      MG(71); TR(73); v_rcp(tm[14]); v_rcp(tm[15]); MU(71); E5(1,14); CV(6); E5(1,15);
      MG(72); TR(74); GISS(2); CV(7); MU(72); HST(1,1); MARK(hA);
      MG(73); TR(75); MU(73);
      MG(74); TR(76); ld_slot_c<wd_slot(30), 30>(rs, dvoff, dcur); MU(74);
      MG(75); TR(77); ld_slot_c<wd_slot(31), 31>(rs, dvoff, dcur); MU(75); adv(dcur);
      MG(76); TR(78); MU(76);
      MG(77); TR(79); ld_slot_c<wd_slot(32), 32>(rs, dvoff, dcur); MU(77);
      MG(78); TR(80); ld_slot_c<wd_slot(33), 33>(rs, dvoff, dcur); MU(78);
      MG(79); TR(81); ld_slot_c<wd_slot(34), 34>(rs, dvoff, dcur); MU(79);
      MG(80); TR(82); MU(80);
      VMW(16); SBAR();
      MG(81); TR(83); RESD(1); MU(81); WAITV(hA, 6); SBAR(); add_one(has, zero, x.w, 1ull);
      MG(82); TR(84); ld_slot_c<wd_slot(35), 35>(rs, dvoff, dcur); MU(82); adv(dcur); GCHK(8, 2);
      MG(83); TR(85); PREP1(); MU(83); PREP2(2); STAMP(12 + 13 * layer);
      AUG(2);
      MG(84); TR(86); ld_slot_c<wd_slot(36), 36>(rs, dvoff, dcur); MU(84);
      MG(85); TR(87); ld_slot_c<wd_slot(37), 37>(rs, dvoff, dcur); MU(85);
      MG(86); TR(88); ld_slot_c<wd_slot(38), 38>(rs, dvoff, dcur); MU(86); E1(2,0); E1(2,1); v_exp(tm[0]); E1(2,2);
      MG(87); TR(89); ld_slot_c<wd_slot(39), 39>(rs, dvoff, dcur); MU(87); adv(dcur); v_exp(tm[1]); v_fma1(tm[0], sd[0]); E1(2,3);
      MG(88); TR(90); ld_slot_c<wd_slot(40), 40>(rs, dvoff, dcur); MU(88); v_exp(tm[2]); v_fma1(tm[1], sd[0]); v_rcp(tm[0]);
      MG(89); TR(91); ld_slot_c<wd_slot(41), 41>(rs, dvoff, dcur); MU(89); E1(2,4); v_exp(tm[3]); v_fma1(tm[2], sd[0]); E5(2,0);
      MG(90); TR(92); ld_slot_c<wd_slot(42), 42>(rs, dvoff, dcur); MU(90); v_rcp(tm[1]); E1(2,5); v_exp(tm[4]);
      MG(91); TR(93); ld_slot_c<wd_slot(43), 43>(rs, dvoff, dcur); MU(91); adv(dcur); v_fma1(tm[3], sd[0]); v_rcp(tm[2]); E5(2,1);
      MG(92); TR(94); ld_slot_c<wd_slot(44), 44>(rs, dvoff, dcur); MU(92); E1(2,6); v_exp(tm[5]); v_fma1(tm[4], sd[0]); E5(2,2);
      MG(93); TR(95); ld_slot_c<wd_slot(45), 45>(rs, dvoff, dcur); MU(93); v_rcp(tm[3]); CV(0); E1(2,7); v_fma1(tm[5], sd[0]);
      MG(94); TR(96); ld_slot_c<wd_slot(46), 46>(rs, dvoff, dcur); MU(94); GISS(3); v_exp(tm[6]);
      MG(95); TR(97); ld_slot_c<wd_slot(47), 47>(rs, dvoff, dcur); MU(95); adv(dcur); v_rcp(tm[4]); E5(2,3); E1(2,8);
      MG(96); TR(98); ld_slot_c<wd_slot(48), 48>(rs, dvoff, dcur); MU(96); poll_issue(pv, hao, zero, x.w); v_fma1(tm[6], sd[0]);
      MG(97); TR(99); ld_slot_c<wd_slot(49), 49>(rs, dvoff, dcur); MU(97); v_exp(tm[7]); v_rcp(tm[5]); E5(2,4);
      MG(98); TR(100); ld_slot_c<wd_slot(50), 50>(rs, dvoff, dcur); MU(98); CV(1); E1(2,9); v_exp(tm[8]); v_fma1(tm[7], sd[0]);
      MG(99); TR(101); ld_slot_c<wd_slot(51), 51>(rs, dvoff, dcur); MU(99); adv(dcur); v_rcp(tm[6]); E5(2,5); E1(2,10);
      MG(100); TR(102); ld_slot_c<wd_slot(52), 52>(rs, dvoff, dcur); MU(100); v_exp(tm[9]); v_fma1(tm[8], sd[0]); v_rcp(tm[7]);
      MG(101); TR(103); ld_slot_c<wd_slot(53), 53>(rs, dvoff, dcur); MU(101); E5(2,6); CV(2); E1(2,11); v_exp(tm[10]);
      MG(102); TR(104); ld_slot_c<wd_slot(54), 54>(rs, dvoff, dcur); MU(102); v_fma1(tm[9], sd[0]); v_rcp(tm[8]); E5(2,7); E1(2,12);
      MG(103); TR(105); ld_slot_c<wd_slot(55), 55>(rs, dvoff, dcur); MU(103); v_exp(tm[11]); v_fma1(tm[10], sd[0]); v_rcp(tm[9]);
      MG(104); TR(106); GCHK(9, 3); E5(2,8); MU(104); CV(3); E1(2,13); v_exp(tm[12]); v_fma1(tm[11], sd[0]);
      MG(105); TR(107); PREP1(); MU(105); HST(2,0);
      MG(106); TR(108); PREP2(3); E5(2,9); MU(106); v_rcp(tm[10]); E1(2,14); v_exp(tm[13]);
      MG(107); TR(109); v_fma1(tm[12], sd[0]); v_rcp(tm[11]); E5(2,10); MU(107); poll_check<8>(pv, hao, zero, 14u, x.w);
      MG(108); TR(110); ld_slot<HS + 0, 0>(rs, dvoff, hA); MU(108); CV(4); E1(2,15); v_exp(tm[14]); v_fma1(tm[13], sd[0]);
      MG(109); TR(111); ld_slot<HS + 1, 1>(rs, dvoff, hA); MU(109); v_rcp(tm[12]); E5(2,11); v_exp(tm[15]);
      MG(110); ld_slot<HS + 2, 2>(rs, dvoff, hA); v_fma1(tm[14], sd[0]); E5(2,12); MU(110); v_rcp(tm[13]); CV(5); v_fma1(tm[15], sd[0]); E5(2,13);
      MG(111); ld_slot<HS + 3, 3>(rs, dvoff, hA); v_rcp(tm[14]); MU(111); v_rcp(tm[15]); E5(2,14); CV(6); E5(2,15); STAMP(13 + 13 * layer);
      AUG(3); CV(7); HST(2,1); ACCWAIT(ag[1], au[1]); P1(0); P1(1); P2(0); P1(2); ld_slot<HS + 4, 4>(rs, dvoff, hA); P2(1); P3(0); P1(3); P2(2); P3(1);
      P4(0); ld_slot<HS + 5, 5>(rs, dvoff, hA); P1(4); P2(3); P3(2); P4(1); P5(0); P1(5); ld_slot<HS + 6, 6>(rs, dvoff, hA); P2(4); P3(3); P4(2); P5(1);
      P6(0); P1(6); ld_slot<HS + 7, 7>(rs, dvoff, hA); P2(5); P3(4); P4(3); P5(2); P6(1); P1(7); ld_slot<HS + 8, 8>(rs, dvoff, hA); P2(6); P3(5); P4(4);
      P5(3); P6(2); P2(7); ld_slot<HS + 9, 9>(rs, dvoff, hA); P3(6); P4(5); P5(4); P6(3); HST(3,0); P3(7); ld_slot<HS + 10, 10>(rs, dvoff, hA); P4(6);
      P5(5); P6(4); P4(7); P5(6); P6(5); ld_slot<HS + 11, 11>(rs, dvoff, hA); P5(7); P6(6); P6(7); HST(3,1); MARK(hB); PINW(); STAMP(14 + 13 * layer);
    }
    if (layer) break;
    Pro pa;
    glu_down<false>(wd1, x, sm, ws, out, rs, stampv, [&](auto i_) AINL {
      constexpr int i = decltype(i_)::value;
      if constexpr (i == 6) { WAITV(xA, PREH - 12 + 7); SBAR(); add_one(ctr(ws, C_XA), x.sigv, x.w, 0xffull); }
      if constexpr (i >= 28 - PREH) RING(18 + i - (28 - PREH));
      if constexpr (i == 27) poll_issue(pv, ctr(ws, C_XA), x.polv, x.w);
    }, [&](auto n_) AINL {
      constexpr int n = decltype(n_)::value;
      if constexpr (n < 8 && (n & 1)) RING(18 + PREH + n / 2);
      if constexpr (n == 8) BARRIER();
      if constexpr (n == 10) poll_check<5>(pv, ctr(ws, C_XA), x.polv, NB / 8, x.w);
      if constexpr (n >= 10 && n < 15) pro_load<n - 10>(pa, rs, x.xvoff, x.t16, WS_X0 + 2 * WS_XSTR, WS_ST2);
    });
    MARK(xB);
    STAMP(16);
    SFOR(i, 22 + PREH, 41) RING(i); SEND
    SFOR(i, 0, 8) RING(i); SEND
    WAITV(xB, 27 - PREH);
    SBAR();
    add_one(ctr(ws, C_XB), x.sigv, x.w, 0xffull);
    pro_finish<false>(pa, base, dskip, x.udst, x.t & 1, [&](auto j_) AINL { RING(8 + decltype(j_)::value); });
    STAMP(17);
    {
      f32x4 acc[8];
      ConvPart<0, 4, 0, 4, true> cA; cA.abase = x.abase; cA.abase8 = x.abase8; cA.bbase = x.bbase;
      ConvPart<4, 8, 0, 4, true> cX; cX.abase = x.abase; cX.abase8 = x.abase8; cX.bbase = x.bbase;
      ConvPart<4, 8, 4, 8, false> cB; cB.abase = x.abase; cB.abase8 = x.abase8; cB.bbase = x.bbase;
      cA.preA();
      BARRIER();
      STAMP(18);
      cA.preB();
      cA.run(acc, [&](auto n_) AINL { constexpr int n = decltype(n_)::value; if constexpr (n % 5 == 2 && n < 30) RING(12 + n / 5); });
      STAMP(52);
      conv_epi_a(acc, x, 0, [&](auto) AINL {});
      asm volatile("s_waitcnt lgkmcnt(0)" ::: "memory");
      cX.preB(); cX.preA();
      SBAR();
      conv_epi_b(base, kscale, x, rs, WS_X0 + 3 * WS_XSTR);
      MARK(cA1);
      poll_issue(pv, ctr(ws, C_XB), x.polv, x.w);
      STAMP(53);
      Pro pb;
      cX.run(acc, [&](auto n_) AINL {
        constexpr int n = decltype(n_)::value;
        if constexpr (n < 30 && n % 5 == 2) RING(41 + n / 5);
        if constexpr (n == 30) poll_check<6>(pv, ctr(ws, C_XB), x.polv, NB / 8, x.w);
        if constexpr (n >= 30 && n < 50 && n % 4 == 2) pro_load<(n - 30) / 4>(pb, rs, x.xvoff, x.t16, WS_X0 + 2 * WS_XSTR + 32 * GSL, WS_ST2 + 16384);
        if constexpr (n == 50) { WAITV(cA1, 11); SBAR(); add_one(ctr(ws, C_CA1), x.sigv, x.w, 0xffull); }
      });
      cB.preA();
      RING(47); RING(48); RING(49);
      STAMP(54);
      WAITV0(none, 3, x.w);
      pro_finish<false>(pb, base, dskip, x.udst + 2048, x.t & 1, [&](auto) AINL {});
      BARRIER();
      STAMP(55);
      cB.preB();
      cB.run(acc, [&](auto n_) AINL {
        constexpr int n = decltype(n_)::value;
        if constexpr (n < 30 && n % 5 == 0) RING(50 + n / 5);
        if constexpr (n == 30) poll_issue(pv, ctr(ws, C_CA1), x.polv, x.w);
      });
      STAMP(56);
      conv_epi_a(acc, x, 1, [&](auto) AINL {});
      asm volatile("s_waitcnt lgkmcnt(0)" ::: "memory");
      poll_check<0>(pv, ctr(ws, C_CA1), x.polv, NB / 8, x.w);
      STAMP(20);
    }
  }
  glu_down<true>(wd1, x, sm, ws, out, rs, stampv, [&](auto) AINL {}, [&](auto n_) AINL { if constexpr (decltype(n_)::value == 8) BARRIER(); });
  STAMP(29); RSTAMP1();
}

extern "C" void kernel_launch(void* const* d_in, const int* in_sizes, int n_in, void* d_out, int out_size, void* d_ws,
                              size_t ws_size, hipStream_t stream) {
  k_fused<<<NB + NH, 256, 0, stream>>>((const u16*)d_in[0], (const u16*)d_in[1], (const u16*)d_in[2], (const u16*)d_in[3],
                                       (const u16*)d_in[4], (const u16*)d_in[5], (const u16*)d_in[6], (const u16*)d_in[8],
                                       (const u16*)d_in[9], (const u16*)d_in[10], (u16*)d_out, (char*)d_ws);
}
```
